```python
import torch

def _softmax(x, axis=-1):
    return torch.softmax(x, dim=axis)

def _gelu(x):
    return torch.nn.functional.gelu(x, approximate='tanh')

def _silu(x):
    return torch.nn.functional.silu(x)

def _sigmoid(x):
    return torch.sigmoid(x)

def _layernorm(x, axis=-1, eps=1e-05):
    m = x.mean(dim=axis, keepdim=True)
    v = x.var(dim=axis, keepdim=True, unbiased=False)
    return (x - m) / torch.sqrt(v + eps)

def _scatter_add(tgt, idx, src, axis=0):
    return tgt.clone().index_add_(axis, idx, src)

def _segment_sum(src, seg, n):
    out = torch.zeros((n,) + src.shape[1:], dtype=src.dtype)
    return out.index_add_(0, seg, src)

def _pad(x, pw):
    flat = tuple((v for pair in reversed(pw) for v in pair))
    return torch.nn.functional.pad(x, flat)
kernel_name = 'gather_scatter__molecular_angular_painn__graph'

def setup_inputs():

    _w = lambda a: (lambda t: t.to(torch.bfloat16) if t.is_floating_point() else t)(torch.as_tensor(a))
    N, deg, D = (12500, 32, 128)
    n_rbf = max(4, 4)
    E = N * deg
    cutoff = 5.0
    freqs = (torch.arange(1, n_rbf + 1) * torch.pi / cutoff).to(float)
    return {'sv': _w((lambda *s: torch.randn(*s, dtype=torch.bfloat16) * 0.4)(N, 4, D) * 0.3), 'pos': _w((lambda *s: torch.randn(*s, dtype=torch.bfloat16) * 3.2)(N, 3) * 1.2), 'src': _w((lambda lo, hi, *s: torch.randint(lo, hi, s))(0, N, E).to(torch.int64)), 'dst': _w((lambda lo, hi, *s: torch.randint(lo, hi, s))(0, N, E).to(torch.int64)), 'freqs': _w(freqs), 'w_phi1': _w((lambda *s: torch.randn(*s, dtype=torch.bfloat16) * 3.2)(D, D) / D ** 0.5), 'w_phi2': _w((lambda *s: torch.randn(*s, dtype=torch.bfloat16) * 3.2)(D, 3 * D) / D ** 0.5), 'w_rbf': _w((lambda *s: torch.randn(*s, dtype=torch.bfloat16) * 3.2)(n_rbf, 3 * D) / n_rbf ** 0.5)}

def reference(sv, pos, src, dst, freqs, w_phi1, w_phi2, w_rbf):
    N, _, D = sv.shape
    E = src.shape[0]
    PI, cutoff = (3.141592653589793, 5.0)
    s, v = (sv[:, 0, :].clone(), sv[:, 1:, :].clone())
    r_vec = pos[src] - pos[dst]
    d = torch.sqrt((r_vec * r_vec).sum(-1) + 1e-08)
    r_hat = r_vec / d[:, None]
    rbf = torch.sin(d[:, None] * freqs[None, :]) / d[:, None]
    fcut = 0.5 * (torch.cos(PI * d / cutoff) + 1.0)
    fcut = torch.where(d < cutoff, fcut, 0.0 * d)
    Wf = rbf @ w_rbf * fcut[:, None]
    phi = (_silu(s @ w_phi1) @ w_phi2)[src]
    m = phi * Wf
    m_s, m_vv, m_vr = (m[:, :D].clone(), m[:, D:2 * D].clone(), m[:, 2 * D:].clone())
    ds = _segment_sum(m_s, dst, N)
    dv_e = m_vv[:, None, :] * v[src] + m_vr[:, None, :] * r_hat[:, :, None]
    dv = _segment_sum(dv_e.reshape(E, 3 * D), dst, N).reshape(N, 3, D)
    return sv + torch.concatenate([ds[:, None, :], dv], axis=1)
```

```cpp
#include <hip/hip_runtime.h>
#include <hip/hip_bf16.h>
#include <stdint.h>

using bf16x8 = __attribute__((ext_vector_type(8))) short;
using f32x4 = __attribute__((ext_vector_type(4))) float;
using u32x4 = __attribute__((ext_vector_type(4))) unsigned;
using u32x2 = __attribute__((ext_vector_type(2))) unsigned;
using i32x4 = __attribute__((ext_vector_type(4))) int;
typedef __bf16 bf16x2_t __attribute__((ext_vector_type(2)));
typedef float f32x2 __attribute__((ext_vector_type(2)));

#define DI __device__ __forceinline__

constexpr int D = 128;
constexpr int D3 = 384;
constexpr int GEMM_ROWS = 64;
constexpr int EPT = 8;
constexpr int CH = 256 * EPT;
constexpr int MAXNB = 1024;
constexpr int K2CAP = 1024;

DI float bf_lo(unsigned u) { return __uint_as_float(u << 16); }
DI float bf_hi(unsigned u) { return __uint_as_float(u & 0xffff0000u); }
DI unsigned pack_bf16(float lo, float hi) {
    f32x2 v = {lo, hi};
    bf16x2_t b = __builtin_convertvector(v, bf16x2_t);
    return __builtin_bit_cast(unsigned, b);
}
DI float silu_f(float x) { return x * __builtin_amdgcn_rcpf(1.0f + __expf(-x)); }

struct Rec {
    unsigned key;
    float c[4];
    float rh[3];
};

DI void gemm_block(int blk, const unsigned short* __restrict__ sv, const unsigned short* __restrict__ w1,
                   const unsigned short* __restrict__ w2, unsigned short* __restrict__ phi, int N,
                   char* smem) {
    const int tid = threadIdx.x;
    const int lane = tid & 63;
    const int w = __builtin_amdgcn_readfirstlane(tid >> 6);
    const int i16 = lane & 15, g = lane >> 4;
    const int row0 = blk * GEMM_ROWS;
    constexpr int R = GEMM_ROWS / 16;
    constexpr int HP = 272;

    bf16x8 a1[2][4];
    {
        const unsigned* p = reinterpret_cast<const unsigned*>(w1) + (8 * g) * (D / 2) + (16 * w + i16);
#pragma unroll
        for (int ks = 0; ks < 4; ++ks) {
            unsigned dw[8];
#pragma unroll
            for (int j = 0; j < 8; ++j) dw[j] = p[(32 * ks + j) * (D / 2)];
            u32x4 t0, t1;
#pragma unroll
            for (int r = 0; r < 4; ++r) {
                t0[r] = __builtin_amdgcn_perm(dw[2 * r + 1], dw[2 * r], 0x05040100u);
                t1[r] = __builtin_amdgcn_perm(dw[2 * r + 1], dw[2 * r], 0x07060302u);
            }
            a1[0][ks] = __builtin_bit_cast(bf16x8, t0);
            a1[1][ks] = __builtin_bit_cast(bf16x8, t1);
        }
    }
    bf16x8 bs[R][4];
#pragma unroll
    for (int r = 0; r < R; ++r) {
        int node = row0 + 16 * r + i16;
        node = node < N ? node : N - 1;
        const bf16x8* p = reinterpret_cast<const bf16x8*>(sv + (size_t)node * (4 * D) + 8 * g);
#pragma unroll
        for (int ks = 0; ks < 4; ++ks) bs[r][ks] = p[4 * ks];
    }
    bf16x8 a2[3][2][4];
#pragma unroll
    for (int pp = 0; pp < 3; ++pp) {
        const unsigned* p = reinterpret_cast<const unsigned*>(w2) + (8 * g) * (D3 / 2) + (16 * (3 * w + pp) + i16);
#pragma unroll
        for (int ks = 0; ks < 4; ++ks) {
            unsigned dw[8];
#pragma unroll
            for (int j = 0; j < 8; ++j) dw[j] = p[(32 * ks + j) * (D3 / 2)];
            u32x4 t0, t1;
#pragma unroll
            for (int r = 0; r < 4; ++r) {
                t0[r] = __builtin_amdgcn_perm(dw[2 * r + 1], dw[2 * r], 0x05040100u);
                t1[r] = __builtin_amdgcn_perm(dw[2 * r + 1], dw[2 * r], 0x07060302u);
            }
            a2[pp][0][ks] = __builtin_bit_cast(bf16x8, t0);
            a2[pp][1][ks] = __builtin_bit_cast(bf16x8, t1);
        }
    }
#pragma unroll
    for (int r = 0; r < R; ++r) {
        f32x4 c0 = {0.f, 0.f, 0.f, 0.f}, c1 = {0.f, 0.f, 0.f, 0.f};
#pragma unroll
        for (int ks = 0; ks < 4; ++ks) {
            c0 = __builtin_amdgcn_mfma_f32_16x16x32_bf16(a1[0][ks], bs[r][ks], c0, 0, 0, 0);
            c1 = __builtin_amdgcn_mfma_f32_16x16x32_bf16(a1[1][ks], bs[r][ks], c1, 0, 0, 0);
        }
        u32x4 o;
#pragma unroll
        for (int q = 0; q < 4; ++q) o[q] = pack_bf16(silu_f(c0[q]), silu_f(c1[q]));
        *reinterpret_cast<u32x4*>(smem + (16 * r + i16) * HP + (32 * w + 8 * g) * 2) = o;
    }
    __syncthreads();
#pragma unroll
    for (int r = 0; r < R; ++r) {
        bf16x8 bh[4];
#pragma unroll
        for (int ks = 0; ks < 4; ++ks)
            bh[ks] = *reinterpret_cast<const bf16x8*>(smem + (16 * r + i16) * HP + (32 * ks + 8 * g) * 2);
        const int node = row0 + 16 * r + i16;
#pragma unroll
        for (int pp = 0; pp < 3; ++pp) {
            f32x4 c0 = {0.f, 0.f, 0.f, 0.f}, c1 = {0.f, 0.f, 0.f, 0.f};
#pragma unroll
            for (int ks = 0; ks < 4; ++ks) {
                c0 = __builtin_amdgcn_mfma_f32_16x16x32_bf16(a2[pp][0][ks], bh[ks], c0, 0, 0, 0);
                c1 = __builtin_amdgcn_mfma_f32_16x16x32_bf16(a2[pp][1][ks], bh[ks], c1, 0, 0, 0);
            }
            u32x4 o;
#pragma unroll
            for (int q = 0; q < 4; ++q) o[q] = pack_bf16(c0[q], c1[q]);
            if (node < N)
                *reinterpret_cast<u32x4*>(phi + (size_t)node * D3 + 32 * (3 * w + pp) + 8 * g) = o;
        }
    }
}

DI void load_pos(const unsigned char* __restrict__ posb, unsigned n, unsigned lim, float& x, float& y, float& z) {
    unsigned off = 6u * n;
    unsigned a = off < lim ? off : lim;
    unsigned long long v;
    __builtin_memcpy(&v, posb + a, 8);
    v >>= (off - a) * 8u;
    x = bf_lo((unsigned)v);
    y = bf_hi((unsigned)v);
    z = bf_lo((unsigned)(v >> 32));
}

DI float sin_acc(float x) { return sinf(x); }

DI void edge_block(int chunk, int nchunks, const int* __restrict__ src, const int* __restrict__ dst,
                   const unsigned char* __restrict__ posb, const unsigned short* __restrict__ freqs,
                   Rec* __restrict__ rec, unsigned* __restrict__ tab, int N, int E, int BN, int NB,
                   char* smem) {
    const int tid = threadIdx.x;
    const int lane = tid & 63;
    const int w = __builtin_amdgcn_readfirstlane(tid >> 6);
    unsigned* it_src = reinterpret_cast<unsigned*>(smem);
    unsigned* it_dst = it_src + CH;
    float* it_rx = reinterpret_cast<float*>(it_dst + CH);
    float* it_ry = it_rx + CH;
    float* it_rz = it_ry + CH;
    unsigned short* it_rank = reinterpret_cast<unsigned short*>(it_rz + CH);
    unsigned short* hist = it_rank + CH;
    unsigned* wsum = reinterpret_cast<unsigned*>(hist + 4 * MAXNB);
    const unsigned lim = 6u * (unsigned)N - 8u;

    for (int i = tid; i < 4 * MAXNB / 2; i += 256) reinterpret_cast<unsigned*>(hist)[i] = 0u;

    const int wbase = chunk * CH + w * (EPT * 64);
    int cnt = 0;
    {
        int es[EPT], ed[EPT];
#pragma unroll
        for (int j = 0; j < EPT / 4; ++j) {
            const int e = wbase + (j * 64 + lane) * 4;
            if (e + 3 < E) {
                i32x4 s4 = *reinterpret_cast<const i32x4*>(src + e);
                i32x4 d4 = *reinterpret_cast<const i32x4*>(dst + e);
#pragma unroll
                for (int q = 0; q < 4; ++q) { es[4 * j + q] = s4[q]; ed[4 * j + q] = d4[q]; }
            } else {
#pragma unroll
                for (int q = 0; q < 4; ++q) {
                    es[4 * j + q] = (e + q < E) ? src[e + q] : -1;
                    ed[4 * j + q] = (e + q < E) ? dst[e + q] : 0;
                }
            }
        }
        float rx[EPT], ry[EPT], rz[EPT];
#pragma unroll
        for (int j = 0; j < EPT; ++j) {
            float ax, ay, az, bx, by, bz;
            load_pos(posb, (unsigned)(es[j] < 0 ? 0 : es[j]), lim, ax, ay, az);
            load_pos(posb, (unsigned)ed[j], lim, bx, by, bz);
            rx[j] = ax - bx; ry[j] = ay - by; rz[j] = az - bz;
        }
#pragma unroll
        for (int j = 0; j < EPT; ++j) {
            const float d2 = rx[j] * rx[j] + ry[j] * ry[j] + rz[j] * rz[j] + 1e-8f;
            const float d = __builtin_sqrtf(d2);
            const bool act = (es[j] >= 0) && !(d >= 5.0f && d < __builtin_inff());
            const unsigned long long m = __ballot(act);
            if (act) {
                const int p = w * (EPT * 64) + cnt + __builtin_amdgcn_mbcnt_hi((unsigned)(m >> 32), __builtin_amdgcn_mbcnt_lo((unsigned)m, 0));
                it_src[p] = (unsigned)es[j];
                it_dst[p] = (unsigned)ed[j];
                it_rx[p] = rx[j]; it_ry[p] = ry[j]; it_rz[p] = rz[j];
            }
            cnt += __popcll(m);
        }
    }
    __syncthreads();

    unsigned short* myhist = hist + w * MAXNB;
    for (int b = 0; b < cnt; b += 64) {
        const int i = b + lane;
        const bool valid = i < cnt;
        const int p = w * (EPT * 64) + i;
        const unsigned bin = valid ? it_dst[p] / (unsigned)BN : 0u;
        unsigned long long peers = __ballot(valid);
#pragma unroll
        for (int bit = 0; bit < 10; ++bit) {
            const bool s = (bin >> bit) & 1u;
            const unsigned long long m = __ballot(s);
            peers &= s ? m : ~m;
        }
        if (valid) {
            const unsigned base = myhist[bin];
            const unsigned below = __builtin_amdgcn_mbcnt_hi((unsigned)(peers >> 32), __builtin_amdgcn_mbcnt_lo((unsigned)peers, 0));
            it_rank[p] = (unsigned short)(base + below);
            const unsigned tot = __popcll(peers);
            if (below + 1 == tot) myhist[bin] = (unsigned short)(base + tot);
        }
    }
    __syncthreads();

    unsigned h[4][4], tot[4], tsum = 0;
#pragma unroll
    for (int q = 0; q < 4; ++q) {
        const int bin = 4 * tid + q;
        tot[q] = 0;
#pragma unroll
        for (int ww = 0; ww < 4; ++ww) { h[q][ww] = hist[ww * MAXNB + bin]; tot[q] += h[q][ww]; }
        tsum += tot[q];
    }
    unsigned incl = tsum;
#pragma unroll
    for (int o = 1; o < 64; o <<= 1) {
        const unsigned v = __shfl_up(incl, o, 64);
        if (lane >= o) incl += v;
    }
    if (lane == 63) wsum[w] = incl;
    __syncthreads();
    unsigned run = incl - tsum;
#pragma unroll
    for (int ww = 0; ww < 4; ++ww) run += (ww < w) ? wsum[ww] : 0u;
#pragma unroll
    for (int q = 0; q < 4; ++q) {
        const int bin = 4 * tid + q;
        if (bin < NB) tab[(size_t)bin * nchunks + chunk] = run | (tot[q] << 16);
        unsigned acc = run;
#pragma unroll
        for (int ww = 0; ww < 4; ++ww) { hist[ww * MAXNB + bin] = (unsigned short)acc; acc += h[q][ww]; }
        run += tot[q];
    }
    __syncthreads();

    float fr[4];
#pragma unroll
    for (int k = 0; k < 4; ++k) fr[k] = bf_lo((unsigned)freqs[k]);
    for (int b = 0; b < cnt; b += 64) {
        const int i = b + lane;
        if (i < cnt) {
            const int p = w * (EPT * 64) + i;
            const unsigned s = it_src[p], dd = it_dst[p];
            const float rx = it_rx[p], ry = it_ry[p], rz = it_rz[p];
            const unsigned bin = dd / (unsigned)BN;
            const unsigned dl = dd - bin * (unsigned)BN;
            const unsigned pos = (unsigned)myhist[bin] + (unsigned)it_rank[p];
            const float d2 = rx * rx + ry * ry + rz * rz + 1e-8f;
            const float d = __builtin_sqrtf(d2);
            const float inv = 1.0f / d;
            float fc = 0.5f * (cosf(3.14159265358979f * d / 5.0f) + 1.0f);
            fc = (d < 5.0f) ? fc : 0.0f * d;
            const float sc = inv * fc;
            u32x4 r0, r1;
            r0[0] = s | (dl << 24);
            r0[1] = __float_as_uint(sin_acc(d * fr[0]) * sc);
            r0[2] = __float_as_uint(sin_acc(d * fr[1]) * sc);
            r0[3] = __float_as_uint(sin_acc(d * fr[2]) * sc);
            r1[0] = __float_as_uint(sin_acc(d * fr[3]) * sc);
            r1[1] = __float_as_uint(rx * inv);
            r1[2] = __float_as_uint(ry * inv);
            r1[3] = __float_as_uint(rz * inv);
            u32x4* o = reinterpret_cast<u32x4*>(rec + (size_t)chunk * CH + pos);
            o[0] = r0;
            o[1] = r1;
        }
    }
}

constexpr int K1_SMEM = 5 * CH * 4 + CH * 2 + 4 * MAXNB * 2 + 64;

__global__ __launch_bounds__(256, 2) void painn_k1(const unsigned short* __restrict__ sv,
                                                   const unsigned char* __restrict__ posb,
                                                   const int* __restrict__ src, const int* __restrict__ dst,
                                                   const unsigned short* __restrict__ freqs,
                                                   const unsigned short* __restrict__ w1,
                                                   const unsigned short* __restrict__ w2,
                                                   unsigned short* __restrict__ phi, Rec* __restrict__ rec,
                                                   unsigned* __restrict__ tab, int N, int E, int BN, int NB,
                                                   int nchunks, int ngemm) {
    __shared__ __attribute__((aligned(16))) char smem[K1_SMEM];
    const int b = blockIdx.x;
    const int mn = ngemm < nchunks ? ngemm : nchunks;
    int role, idx;
    if (b < 2 * mn) { role = b & 1; idx = b >> 1; }
    else { role = (ngemm < nchunks) ? 1 : 0; idx = b - mn; }
    if (role == 0) gemm_block(idx, sv, w1, w2, phi, N, smem);
    else edge_block(idx, nchunks, src, dst, posb, freqs, rec, tab, N, E, BN, NB, smem);
}

struct Acc { float s0, s1, x0, x1, y0, y1, z0, z1; };

DI void edge_fma(Acc& a, const float (&wr)[3][4][2], const u32x4 r0, const u32x4 r1, unsigned ps, unsigned pvv,
                 unsigned pvr, unsigned vx, unsigned vy, unsigned vz) {
    const float c0 = __uint_as_float(r0[1]), c1 = __uint_as_float(r0[2]), c2 = __uint_as_float(r0[3]),
                c3 = __uint_as_float(r1[0]);
    const float hx = __uint_as_float(r1[1]), hy = __uint_as_float(r1[2]), hz = __uint_as_float(r1[3]);
    float wf[3][2];
#pragma unroll
    for (int sg = 0; sg < 3; ++sg)
#pragma unroll
        for (int h = 0; h < 2; ++h)
            wf[sg][h] = c0 * wr[sg][0][h] + c1 * wr[sg][1][h] + c2 * wr[sg][2][h] + c3 * wr[sg][3][h];
    a.s0 += bf_lo(ps) * wf[0][0];
    a.s1 += bf_hi(ps) * wf[0][1];
    const float mv0 = bf_lo(pvv) * wf[1][0], mv1 = bf_hi(pvv) * wf[1][1];
    const float mr0 = bf_lo(pvr) * wf[2][0], mr1 = bf_hi(pvr) * wf[2][1];
    a.x0 += mv0 * bf_lo(vx); a.x1 += mv1 * bf_hi(vx);
    a.y0 += mv0 * bf_lo(vy); a.y1 += mv1 * bf_hi(vy);
    a.z0 += mv0 * bf_lo(vz); a.z1 += mv1 * bf_hi(vz);
    a.x0 += mr0 * hx; a.x1 += mr1 * hx;
    a.y0 += mr0 * hy; a.y1 += mr1 * hy;
    a.z0 += mr0 * hz; a.z1 += mr1 * hz;
}

__global__ __launch_bounds__(256) void painn_k2(const unsigned* __restrict__ sv32, const unsigned* __restrict__ phi32,
                                                const unsigned short* __restrict__ wrbf, const Rec* __restrict__ rec,
                                                const unsigned* __restrict__ tab, unsigned* __restrict__ out32, int N,
                                                int BN, int nchunks) {
    __shared__ __attribute__((aligned(16))) u32x4 lrec[K2CAP * 2];
    __shared__ unsigned lsrc[K2CAP];
    __shared__ unsigned loff[1024 + 1];
    __shared__ unsigned lstart[1024];
    __shared__ unsigned wsum[4];
    const int tid = threadIdx.x;
    const int lane = tid & 63;
    const int w = __builtin_amdgcn_readfirstlane(tid >> 6);
    const int bin = blockIdx.x;
    const int node0 = bin * BN;
    const int nn = min(BN, N - node0);

    float wr[3][4][2];
#pragma unroll
    for (int sg = 0; sg < 3; ++sg)
#pragma unroll
        for (int k = 0; k < 4; ++k) {
            const unsigned u = reinterpret_cast<const unsigned*>(wrbf)[(k * D3 + sg * D) / 2 + lane];
            wr[sg][k][0] = bf_lo(u);
            wr[sg][k][1] = bf_hi(u);
        }

    unsigned cn[4], tsum = 0;
#pragma unroll
    for (int q = 0; q < 4; ++q) {
        const int c = 4 * tid + q;
        const unsigned t = (c < nchunks) ? tab[(size_t)bin * nchunks + c] : 0u;
        cn[q] = t >> 16;
        lstart[c] = t & 0xffffu;
        tsum += cn[q];
    }
    unsigned incl = tsum;
#pragma unroll
    for (int o = 1; o < 64; o <<= 1) {
        const unsigned v = __shfl_up(incl, o, 64);
        if (lane >= o) incl += v;
    }
    if (lane == 63) wsum[w] = incl;
    __syncthreads();
    unsigned run = incl - tsum;
#pragma unroll
    for (int ww = 0; ww < 4; ++ww) run += (ww < w) ? wsum[ww] : 0u;
    const int T = (int)(wsum[0] + wsum[1] + wsum[2] + wsum[3]);
    unsigned myoff[4];
#pragma unroll
    for (int q = 0; q < 4; ++q) {
        myoff[q] = run;
        run += cn[q];
    }
    const int nbatch = (T + K2CAP - 1) / K2CAP;
    const int npass = (nn + 3) / 4;

    for (int pass = 0; pass < npass; ++pass) {
        const int j = pass * 4 + w;
        const bool have = j < nn;
        const int node = node0 + (have ? j : 0);
        unsigned o0 = sv32[(size_t)node * 256 + lane], o1 = sv32[(size_t)node * 256 + 64 + lane],
                 o2 = sv32[(size_t)node * 256 + 128 + lane], o3 = sv32[(size_t)node * 256 + 192 + lane];
        Acc a = {0.f, 0.f, 0.f, 0.f, 0.f, 0.f, 0.f, 0.f};
        for (int bt = 0; bt < nbatch; ++bt) {
            const int b0 = bt * K2CAP;
            const int nb = min(K2CAP, T - b0);
            if (nbatch > 1 || pass == 0) {
                __syncthreads();
#pragma unroll
                for (int q = 0; q < 4; ++q) {
                    const int c = 4 * tid + q;
                    const unsigned st = lstart[c];
                    for (unsigned i = 0; i < cn[q]; ++i) {
                        const int it = (int)(myoff[q] + i) - b0;
                        if (it >= 0 && it < nb) lsrc[it] = (unsigned)c * CH + st + i;
                    }
                }
                __syncthreads();
                for (int i = tid; i < 2 * nb; i += 256)
                    lrec[i] = reinterpret_cast<const u32x4*>(rec)[(size_t)lsrc[i >> 1] * 2 + (i & 1)];
                __syncthreads();
            }
            if (have) {
                for (int b = 0; b < nb; b += 64) {
                    const int i = b + lane;
                    const unsigned key = (i < nb) ? lrec[2 * i][0] : 0xffffffffu;
                    unsigned long long m = __ballot((key >> 24) == (unsigned)j && i < nb);
                    while (m) {
                        const int i0 = b + __builtin_ctzll(m);
                        m &= m - 1;
                        if (m) {
                            const int i1 = b + __builtin_ctzll(m);
                            m &= m - 1;
                            const u32x4 ra0 = lrec[2 * i0], ra1 = lrec[2 * i0 + 1];
                            const u32x4 rb0 = lrec[2 * i1], rb1 = lrec[2 * i1 + 1];
                            const unsigned sa = ra0[0] & 0xffffffu, sb = rb0[0] & 0xffffffu;
                            const unsigned* pa = phi32 + (size_t)sa * 192 + lane;
                            const unsigned* va = sv32 + (size_t)sa * 256 + 64 + lane;
                            const unsigned* pb = phi32 + (size_t)sb * 192 + lane;
                            const unsigned* vb = sv32 + (size_t)sb * 256 + 64 + lane;
                            const unsigned a0 = pa[0], a1 = pa[64], a2 = pa[128], a3 = va[0], a4 = va[64], a5 = va[128];
                            const unsigned b0_ = pb[0], b1 = pb[64], b2 = pb[128], b3 = vb[0], b4 = vb[64], b5 = vb[128];
                            edge_fma(a, wr, ra0, ra1, a0, a1, a2, a3, a4, a5);
                            edge_fma(a, wr, rb0, rb1, b0_, b1, b2, b3, b4, b5);
                        } else {
                            const u32x4 ra0 = lrec[2 * i0], ra1 = lrec[2 * i0 + 1];
                            const unsigned sa = ra0[0] & 0xffffffu;
                            const unsigned* pa = phi32 + (size_t)sa * 192 + lane;
                            const unsigned* va = sv32 + (size_t)sa * 256 + 64 + lane;
                            const unsigned a0 = pa[0], a1 = pa[64], a2 = pa[128], a3 = va[0], a4 = va[64], a5 = va[128];
                            edge_fma(a, wr, ra0, ra1, a0, a1, a2, a3, a4, a5);
                        }
                    }
                }
            }
        }
        if (have) {
            unsigned* o = out32 + (size_t)node * 256 + lane;
            o[0] = pack_bf16(bf_lo(o0) + a.s0, bf_hi(o0) + a.s1);
            o[64] = pack_bf16(bf_lo(o1) + a.x0, bf_hi(o1) + a.x1);
            o[128] = pack_bf16(bf_lo(o2) + a.y0, bf_hi(o2) + a.y1);
            o[192] = pack_bf16(bf_lo(o3) + a.z0, bf_hi(o3) + a.z1);
        }
    }
}

extern "C" void kernel_launch(void* const* d_in, const int* in_sizes, int n_in, void* d_out, int out_size,
                              void* d_ws, size_t ws_size, hipStream_t stream) {
    const unsigned short* sv = (const unsigned short*)d_in[0];
    const unsigned char* pos = (const unsigned char*)d_in[1];
    const int* src = (const int*)d_in[2];
    const int* dst = (const int*)d_in[3];
    const unsigned short* freqs = (const unsigned short*)d_in[4];
    const unsigned short* w1 = (const unsigned short*)d_in[5];
    const unsigned short* w2 = (const unsigned short*)d_in[6];
    const unsigned short* wrbf = (const unsigned short*)d_in[7];
    const int N = in_sizes[0] / (4 * D);
    const int E = in_sizes[2];

    int BN = 16;
    while ((N + BN - 1) / BN > MAXNB) BN *= 2;
    const int NB = (N + BN - 1) / BN;
    const int nchunks = (E + CH - 1) / CH;
    const int ngemm = (N + GEMM_ROWS - 1) / GEMM_ROWS;

    char* ws = (char*)d_ws;
    unsigned short* phi = (unsigned short*)ws;
    size_t off = ((size_t)N * D3 * 2 + 255) & ~(size_t)255;
    Rec* rec = (Rec*)(ws + off);
    off += (size_t)nchunks * CH * sizeof(Rec);
    unsigned* tab = (unsigned*)(ws + off);

    painn_k1<<<ngemm + nchunks, 256, 0, stream>>>(sv, pos, src, dst, freqs, w1, w2, phi, rec, tab, N, E, BN, NB,
                                                  nchunks, ngemm);
    painn_k2<<<NB, 256, 0, stream>>>((const unsigned*)sv, (const unsigned*)phi, wrbf, rec, tab, (unsigned*)d_out, N, BN,
                                     nchunks);
}
```

```cpp
#include <hip/hip_runtime.h>
#include <hip/hip_bf16.h>
#include <stdint.h>

using bf16x8 = __attribute__((ext_vector_type(8))) short;
using f32x4 = __attribute__((ext_vector_type(4))) float;
using u32x4 = __attribute__((ext_vector_type(4))) unsigned;
using u32x2 = __attribute__((ext_vector_type(2))) unsigned;
using i32x4 = __attribute__((ext_vector_type(4))) int;
typedef __bf16 bf16x2_t __attribute__((ext_vector_type(2)));
typedef float f32x2 __attribute__((ext_vector_type(2)));

#define DI __device__ __forceinline__
#define LAM __attribute__((always_inline))
DI float fma_s(float a, float b, float c) { float d; asm("v_fma_f32 %0, %1, %2, %3" : "=v"(d) : "v"(a), "v"(b), "v"(c)); return d; }
DI float mul_s(float a, float b) { float d; asm("v_mul_f32 %0, %1, %2" : "=v"(d) : "v"(a), "v"(b)); return d; }
DI void masked_copy8(float* d, const float* s, unsigned long long mask) {
    unsigned long long sv;
    asm volatile("s_and_saveexec_b64 %8, %17\n\ts_cbranch_execz .Lmc%=\n\t"
                 "v_mov_b32 %0, %9\n\tv_mov_b32 %1, %10\n\tv_mov_b32 %2, %11\n\tv_mov_b32 %3, %12\n\t"
                 "v_mov_b32 %4, %13\n\tv_mov_b32 %5, %14\n\tv_mov_b32 %6, %15\n\tv_mov_b32 %7, %16\n"
                 ".Lmc%=:\n\ts_mov_b64 exec, %8"
                 : "+v"(d[0]), "+v"(d[1]), "+v"(d[2]), "+v"(d[3]), "+v"(d[4]), "+v"(d[5]), "+v"(d[6]), "+v"(d[7]), "=&s"(sv)
                 : "v"(s[0]), "v"(s[1]), "v"(s[2]), "v"(s[3]), "v"(s[4]), "v"(s[5]), "v"(s[6]), "v"(s[7]), "s"(mask)
                 : "scc");
}
DI void masked_zero16(float* d, unsigned long long mask) {
    unsigned long long sv;
    asm volatile("s_and_saveexec_b64 %16, %17\n\ts_cbranch_execz .Lmz%=\n\t"
                 "v_mov_b32 %0, 0\n\tv_mov_b32 %1, 0\n\tv_mov_b32 %2, 0\n\tv_mov_b32 %3, 0\n\t"
                 "v_mov_b32 %4, 0\n\tv_mov_b32 %5, 0\n\tv_mov_b32 %6, 0\n\tv_mov_b32 %7, 0\n\t"
                 "v_mov_b32 %8, 0\n\tv_mov_b32 %9, 0\n\tv_mov_b32 %10, 0\n\tv_mov_b32 %11, 0\n\t"
                 "v_mov_b32 %12, 0\n\tv_mov_b32 %13, 0\n\tv_mov_b32 %14, 0\n\tv_mov_b32 %15, 0\n"
                 ".Lmz%=:\n\ts_mov_b64 exec, %16"
                 : "+v"(d[0]), "+v"(d[1]), "+v"(d[2]), "+v"(d[3]), "+v"(d[4]), "+v"(d[5]), "+v"(d[6]), "+v"(d[7]),
                   "+v"(d[8]), "+v"(d[9]), "+v"(d[10]), "+v"(d[11]), "+v"(d[12]), "+v"(d[13]), "+v"(d[14]), "+v"(d[15]), "=&s"(sv)
                 : "s"(mask) : "scc");
}
DI void fmac_s(float& d, float a, float b) { asm("v_fmac_f32 %0, %1, %2" : "+v"(d) : "v"(a), "v"(b)); }
#define STAMP(i)
#define STAMP2(i)
#define STAMPW(i)
#define STAMPW16()
#define STAMPX(i)

constexpr int D = 128;
constexpr int D3 = 384;

DI float bf_lo(unsigned u) { return __uint_as_float(u << 16); }
DI float bf_hi(unsigned u) { return __uint_as_float(u & 0xffff0000u); }
DI unsigned pack_bf16(float lo, float hi) {
    f32x2 v = {lo, hi};
    bf16x2_t b = __builtin_convertvector(v, bf16x2_t);
    return __builtin_bit_cast(unsigned, b);
}
DI float silu_f(float x) { return x * __builtin_amdgcn_rcpf(1.0f + __expf(-x)); }

struct Rec {
    unsigned key;
    float c[4];
    float rh[3];
};

typedef __attribute__((address_space(3))) void* lds_ptr_t;
typedef short s16x4 __attribute__((ext_vector_type(4)));
typedef __attribute__((address_space(3))) s16x4* lds_s16x4_ptr;

DI unsigned wave_incl_scan(unsigned v, int lane) {
    v += (unsigned)__builtin_amdgcn_update_dpp(0, (int)v, 0x111, 0xf, 0xf, false);
    v += (unsigned)__builtin_amdgcn_update_dpp(0, (int)v, 0x112, 0xf, 0xf, false);
    v += (unsigned)__builtin_amdgcn_update_dpp(0, (int)v, 0x114, 0xf, 0xf, false);
    v += (unsigned)__builtin_amdgcn_update_dpp(0, (int)v, 0x118, 0xf, 0xf, false);
    v += (unsigned)__builtin_amdgcn_update_dpp(0, (int)v, 0x142, 0xa, 0xf, false);
    v += (unsigned)__builtin_amdgcn_update_dpp(0, (int)v, 0x143, 0xc, 0xf, false);
    return v;
}
DI unsigned mbcnt64(unsigned long long m) {
    return __builtin_amdgcn_mbcnt_hi((unsigned)(m >> 32), __builtin_amdgcn_mbcnt_lo((unsigned)m, 0));
}
DI void dma16(const __amdgpu_buffer_rsrc_t rsrc, unsigned voff, unsigned lds_dst) {
    unsigned keep;
    asm volatile("s_mov_b32 %0, m0\n\ts_mov_b32 m0, %3\n\ts_nop 0\n\tbuffer_load_dwordx4 %1, %2, 0 offen lds\n\ts_mov_b32 m0, %0"
                 : "=&s"(keep) : "v"(voff), "s"(rsrc), "s"(lds_dst) : "memory");
}
DI unsigned lds_addr(const void* p) { return (unsigned)(size_t)(lds_ptr_t)p; }

DI unsigned pack_hi_comp(float p, unsigned lo16) {
    const unsigned u = __float_as_uint(p);
    const unsigned t = __builtin_elementwise_sub_sat((u & 0x7fffffffu) + 0x8000u, lo16);
    return (u & 0x80000000u) | (t & 0x7fff0000u) | lo16;
}

constexpr int K1_THREADS = 512;
constexpr int G_TILES = 5;
constexpr int G_ROWS = 16 * G_TILES;
constexpr int HP = 272;
constexpr int GO_S = 131072;
constexpr int GO_H = 0;
constexpr int GO_V = 32768;
constexpr int GO_OST = 98304;
constexpr int K1_SMEM = 163840;
#define ST_AUX 16

DI unsigned wswz(unsigned row) { return ((row & 3u) << 2) | ((row >> 2) & 3u); }

DI void gemm_block(int blk, const unsigned short* __restrict__ sv, const unsigned short* __restrict__ w1,
                   const unsigned short* __restrict__ w2, unsigned char* __restrict__ TE, int N, char* smem) {
    const int tid = threadIdx.x;
    const int lane = tid & 63;
    const int w = __builtin_amdgcn_readfirstlane(tid >> 6);
    const int i16 = lane & 15, g = lane >> 4;
    const int row0 = blk * G_ROWS;
    const unsigned sm = lds_addr(smem);
    STAMP(0);
    const __amdgpu_buffer_rsrc_t rW1 = __builtin_amdgcn_make_buffer_rsrc((void*)w1, 0, D * D * 2, 0x00020000);
    const __amdgpu_buffer_rsrc_t rW2 = __builtin_amdgcn_make_buffer_rsrc((void*)w2, 0, D * D3 * 2, 0x00020000);
    const __amdgpu_buffer_rsrc_t rSV = __builtin_amdgcn_make_buffer_rsrc((void*)sv, 0, (unsigned)N * 1024u, 0x00020000);
    const unsigned rot = (unsigned)blk * 5u;

#pragma unroll
    for (int q = 0; q < 4; ++q) {
        const unsigned i = ((unsigned)(w + 8 * q) + rot) & 31u;
        const unsigned row = 4u * i + (unsigned)g;
        dma16(rW1, row * 256u + (((unsigned)i16 ^ wswz(row)) * 16u), sm + i * 1024u);
    }
#pragma unroll
    for (int q = 0; q < 3; ++q) {
        const unsigned i = (unsigned)(w + 8 * q);
        if (i < G_ROWS / 4) {
            const unsigned node = 4u * i + (unsigned)g;
            dma16(rSV, ((unsigned)row0 + node) * 1024u + (((unsigned)i16 ^ (node & 15u)) * 16u), sm + GO_S + i * 1024u);
        }
    }
#pragma unroll
    for (int q = 0; q < 12; ++q) {
        const unsigned i = ((unsigned)(w + 8 * (q & 3)) + rot) & 31u, seg = (unsigned)(q >> 2);
        const unsigned row = 4u * i + (unsigned)g;
        dma16(rW2, row * 768u + seg * 256u + (((unsigned)i16 ^ wswz(row)) * 16u), sm + 32768u * (seg + 1u) + i * 1024u);
    }
    u32x4 vreg[8];
#pragma unroll
    for (int q = 0; q < 8; ++q) {
        const unsigned c = (unsigned)(tid + K1_THREADS * q);
        const unsigned node = __umulhi(c, 89478486u), cc = c - node * 48u;
        vreg[q] = __builtin_amdgcn_raw_buffer_load_b128(rSV, node < G_ROWS ? ((unsigned)row0 + node) * 1024u + 256u + cc * 16u : 0x80000000u, 0, 0);
    }
    asm volatile("s_waitcnt vmcnt(20)" ::: "memory");
    __syncthreads();
    STAMP(1);
    const unsigned tq = (unsigned)i16 >> 2, tp = (unsigned)lane & 3u;
    auto afrag = [&](int im, int ks) LAM {
        const unsigned row = 32u * ks + 8u * g + tq;
        const s16x4 lo = __builtin_amdgcn_ds_read_tr16_b64_v4i16((lds_s16x4_ptr)(smem + im * 32768 + 256u * row +
                            16u * ((2u * w + (tp >> 1)) ^ wswz(row)) + 8u * (tp & 1u)));
        const s16x4 hi = __builtin_amdgcn_ds_read_tr16_b64_v4i16((lds_s16x4_ptr)(smem + im * 32768 + 256u * (row + 4u) +
                            16u * ((2u * w + (tp >> 1)) ^ wswz(row + 4u)) + 8u * (tp & 1u)));
        return bf16x8{lo[0], lo[1], lo[2], lo[3], hi[0], hi[1], hi[2], hi[3]};
    };
    bf16x8 a1[4];
#pragma unroll
    for (int ks = 0; ks < 4; ++ks) a1[ks] = afrag(0, ks);
    __syncthreads();
#pragma unroll
    for (int t = 0; t < G_TILES; ++t) {
        const char* st = smem + GO_S + (16 * t + i16) * 256;
        f32x4 c = {0.f, 0.f, 0.f, 0.f};
#pragma unroll
        for (int ks = 0; ks < 4; ++ks) {
            const bf16x8 b = *reinterpret_cast<const bf16x8*>(st + (((unsigned)(4 * ks + g) ^ (unsigned)i16) * 16u));
            c = __builtin_amdgcn_mfma_f32_16x16x32_bf16(a1[ks], b, c, 0, 0, 0);
        }
        u32x2 o;
        o[0] = pack_bf16(silu_f(c[0]), silu_f(c[1]));
        o[1] = pack_bf16(silu_f(c[2]), silu_f(c[3]));
        *reinterpret_cast<u32x2*>(smem + GO_H + (16 * t + i16) * HP + (16 * w + 4 * g) * 2) = o;
    }
    STAMP(2);
    asm volatile("s_waitcnt vmcnt(0)" ::: "memory");
    __syncthreads();
    bf16x8 a2[3][4];
#pragma unroll
    for (int q = 0; q < 3; ++q)
#pragma unroll
        for (int ks = 0; ks < 4; ++ks) a2[q][ks] = afrag(q + 1, ks);
    __syncthreads();
#pragma unroll
    for (int q = 0; q < 8; ++q) {
        const unsigned c = (unsigned)(tid + K1_THREADS * q);
        const unsigned node = __umulhi(c, 89478486u), cc = c - node * 48u;
        if (node < G_ROWS)
            *reinterpret_cast<u32x4*>(smem + GO_V + node * 768u + (cc & 48u) * 16u + (((cc & 15u) ^ (node & 15u)) * 16u)) = vreg[q];
    }
    __syncthreads();
    STAMP(3);
    const __amdgpu_buffer_rsrc_t rT =
        __builtin_amdgcn_make_buffer_rsrc((void*)(TE + (size_t)w * N * 224), 0, (unsigned)N * 192u, 0x00020000);
    const __amdgpu_buffer_rsrc_t rS =
        __builtin_amdgcn_make_buffer_rsrc((void*)(TE + (size_t)w * N * 224 + (size_t)N * 192), 0, (unsigned)N * 32u, 0x00020000);
    char* ost = smem + GO_OST + w * 3072;
    const unsigned sw = ((((unsigned)(2 * w + (g >> 1))) ^ (unsigned)i16) * 16u) + (unsigned)(g & 1) * 8u;
#pragma unroll 1
    for (int t = 0; t < G_TILES; ++t) {
        bf16x8 bh[4];
#pragma unroll
        for (int ks = 0; ks < 4; ++ks)
            bh[ks] = *reinterpret_cast<const bf16x8*>(smem + GO_H + (16 * t + i16) * HP + (32 * ks + 8 * g) * 2);
        const u32x2 s2 = *reinterpret_cast<const u32x2*>(smem + GO_S + (16 * t + i16) * 256 + sw);
        const char* st = smem + GO_V + (16 * t + i16) * 768 + sw;
#pragma unroll
        for (int q = 0; q < 3; ++q) {
            f32x4 c = {0.f, 0.f, 0.f, 0.f};
#pragma unroll
            for (int ks = 0; ks < 4; ++ks) c = __builtin_amdgcn_mfma_f32_16x16x32_bf16(a2[q][ks], bh[ks], c, 0, 0, 0);
            const u32x2 v2 = *reinterpret_cast<const u32x2*>(st + q * 256);
            u32x4 o;
            o[0] = pack_hi_comp(c[0], v2[0] & 0xffffu);
            o[1] = pack_hi_comp(c[1], v2[0] >> 16);
            o[2] = pack_hi_comp(c[2], v2[1] & 0xffffu);
            o[3] = pack_hi_comp(c[3], v2[1] >> 16);
            *reinterpret_cast<u32x4*>(ost + i16 * 192 + q * 64 + g * 16) = o;
        }
        const unsigned nb = (unsigned)(row0 + 16 * t);
#pragma unroll
        for (int q = 0; q < 3; ++q) {
            const u32x4 o = *reinterpret_cast<const u32x4*>(ost + q * 1024 + lane * 16);
            __builtin_amdgcn_raw_buffer_store_b128(o, rT, nb * 192u + (unsigned)(q * 1024 + lane * 16), 0, ST_AUX);
        }
        __builtin_amdgcn_raw_buffer_store_b64(s2, rS, (nb + (unsigned)i16) * 32u + (unsigned)g * 8u, 0, ST_AUX);
        if (t == 0) STAMP(4);
    }
    STAMP(5);
}

constexpr int EPT = 8;
constexpr int CH = K1_THREADS * EPT;
constexpr int MAXNB = 64;
constexpr int EO_POS = 0;
constexpr int POS_LDS_MAX = 98304;
constexpr int EO_SRC = POS_LDS_MAX;
constexpr int EO_DST = EO_SRC + CH * 4;
constexpr int EO_RANK = EO_DST + CH * 4;
constexpr int EO_HIST = EO_RANK + CH * 2;
static_assert(EO_HIST + 8 * MAXNB * 4 <= K1_SMEM, "LDS");

struct Pos3 { float x, y, z; };
DI Pos3 unpack_pos(unsigned a, unsigned b, unsigned odd16) {
    const unsigned lo = __builtin_amdgcn_alignbit(b, a, odd16);
    const unsigned hi = b >> odd16;
    return Pos3{bf_lo(lo), bf_hi(lo), bf_lo(hi)};
}
DI Pos3 pos_lds(const char* lp, unsigned n) {
    const unsigned off = 6u * n;
    const unsigned* p = reinterpret_cast<const unsigned*>(lp + (off & ~3u));
    return unpack_pos(p[0], p[1], (off & 2u) << 3);
}
DI Pos3 pos_glb(const unsigned char* __restrict__ posb, unsigned n, unsigned lim) {
    const unsigned off = 6u * n;
    const unsigned a = off < lim ? off : lim;
    unsigned long long v;
    __builtin_memcpy(&v, posb + a, 8);
    v >>= (off - a) * 8u;
    return Pos3{bf_lo((unsigned)v), bf_hi((unsigned)v), bf_lo((unsigned)(v >> 32))};
}
DI float sin_rev(float r) { return __builtin_amdgcn_sinf(r - __builtin_rintf(r)); }
DI float cos_rev(float r) { return __builtin_amdgcn_cosf(r - __builtin_rintf(r)); }

template <bool POS_LDS>
DI void edge_block(int chunk, int nchunks, const int* __restrict__ src, const int* __restrict__ dst,
                   const unsigned char* __restrict__ posb, const unsigned short* __restrict__ freqs,
                   Rec* __restrict__ rec, unsigned* __restrict__ tab, int N, int E, int BN, int NB, unsigned binmagic,
                   char* smem) {
    const int tid = threadIdx.x;
    const int lane = tid & 63;
    const int w = __builtin_amdgcn_readfirstlane(tid >> 6);
    unsigned* it_src = reinterpret_cast<unsigned*>(smem + EO_SRC);
    unsigned* it_dst = reinterpret_cast<unsigned*>(smem + EO_DST);
    unsigned short* it_rank = reinterpret_cast<unsigned short*>(smem + EO_RANK);
    unsigned* hist = reinterpret_cast<unsigned*>(smem + EO_HIST);
    const unsigned lim = 6u * (unsigned)N - 8u;
    STAMP(0);
    const int wbase = chunk * CH + w * (EPT * 64);
    int es[EPT], ed[EPT];
#pragma unroll
    for (int j = 0; j < EPT / 4; ++j) {
        const int e = wbase + (j * 64 + lane) * 4;
        if (e + 3 < E) {
            const i32x4 s4 = *reinterpret_cast<const i32x4*>(src + e);
            const i32x4 d4 = *reinterpret_cast<const i32x4*>(dst + e);
#pragma unroll
            for (int q = 0; q < 4; ++q) { es[4 * j + q] = s4[q]; ed[4 * j + q] = d4[q]; }
        } else {
#pragma unroll
            for (int q = 0; q < 4; ++q) {
                es[4 * j + q] = (e + q < E) ? src[e + q] : -1;
                ed[4 * j + q] = (e + q < E) ? dst[e + q] : 0;
            }
        }
    }
    if (POS_LDS) {
        const unsigned bytes = 6u * (unsigned)N;
        const __amdgpu_buffer_rsrc_t rP = __builtin_amdgcn_make_buffer_rsrc((void*)posb, 0, bytes & ~3u, 0x00020000);
        const unsigned sm = lds_addr(smem);
        for (unsigned o = (unsigned)w * 1024u; o < bytes; o += 8192u) dma16(rP, o + (unsigned)lane * 16u, sm + EO_POS + o);
    }
    hist[tid] = 0;
    if (POS_LDS) {
        asm volatile("s_waitcnt vmcnt(0)" ::: "memory");
        __syncthreads();
        if ((N & 1) && tid == 0)
            *reinterpret_cast<unsigned*>(smem + EO_POS + ((6u * (unsigned)N) & ~3u)) =
                (unsigned)*reinterpret_cast<const unsigned short*>(posb + 6u * (unsigned)N - 2u);
        if (N & 1) __syncthreads();
    } else {
        __syncthreads();
    }
    STAMP(1);
    int cnt = 0;
#pragma unroll
    for (int j = 0; j < EPT; ++j) {
        const unsigned s = (unsigned)(es[j] < 0 ? 0 : es[j]), d = (unsigned)ed[j];
        const Pos3 a = POS_LDS ? pos_lds(smem + EO_POS, s) : pos_glb(posb, s, lim);
        const Pos3 b = POS_LDS ? pos_lds(smem + EO_POS, d) : pos_glb(posb, d, lim);
        const float rx = a.x - b.x, ry = a.y - b.y, rz = a.z - b.z;
        const float d2 = rx * rx + ry * ry + rz * rz + 1e-8f;
        const bool act = (es[j] >= 0) && !(d2 >= 25.0f && d2 < __builtin_inff());
        const unsigned long long m = __ballot(act);
        if (act) {
            const int p = w * (EPT * 64) + cnt + (int)mbcnt64(m);
            it_src[p] = s;
            it_dst[p] = d;
        }
        cnt += __popcll(m);
    }
    STAMP(2);
    unsigned* myhist = hist + w * MAXNB;
#pragma unroll 1
    for (int b = lane; b < cnt; b += 64) {
        const int p = w * (EPT * 64) + b;
        it_rank[p] = (unsigned short)atomicAdd(&myhist[__umulhi(it_dst[p], binmagic)], 1u);
    }
    __syncthreads();
    if (w == 0) {
        unsigned h[8], tot = 0;
#pragma unroll
        for (int ww = 0; ww < 8; ++ww) { h[ww] = hist[ww * MAXNB + lane]; tot += h[ww]; }
        unsigned run = wave_incl_scan(tot, lane) - tot;
        if (lane < NB) tab[(size_t)lane * nchunks + chunk] = run | (tot << 16);
#pragma unroll
        for (int ww = 0; ww < 8; ++ww) { hist[ww * MAXNB + lane] = run; run += h[ww]; }
    }
    __syncthreads();
    STAMP(3);
    float fr[4];
#pragma unroll
    for (int k = 0; k < 4; ++k) fr[k] = bf_lo((unsigned)freqs[k]) * 0.15915494309189535f;
#pragma unroll 1
    for (int b = lane; b < cnt; b += 64) {
        const int p = w * (EPT * 64) + b;
        const unsigned s = it_src[p], dd = it_dst[p];
        const Pos3 a = POS_LDS ? pos_lds(smem + EO_POS, s) : pos_glb(posb, s, lim);
        const Pos3 c = POS_LDS ? pos_lds(smem + EO_POS, dd) : pos_glb(posb, dd, lim);
        const float rx = a.x - c.x, ry = a.y - c.y, rz = a.z - c.z;
        const unsigned bin = __umulhi(dd, binmagic);
        const unsigned dl = dd - bin * (unsigned)BN;
        const unsigned pos = myhist[bin] + (unsigned)it_rank[p];
        const float d2 = rx * rx + ry * ry + rz * rz + 1e-8f;
        const float d = __builtin_sqrtf(d2);
        const float inv = 1.0f / d;
        float fc = 0.5f * (cos_rev(d * 0.1f) + 1.0f);
        fc = (d < 5.0f) ? fc : 0.0f * d;
        const float sc = inv * fc;
        u32x4 r0, r1;
        r0[0] = s | (dl << 22);
        r0[1] = __float_as_uint(rx * inv);
        r0[2] = __float_as_uint(ry * inv);
        r0[3] = __float_as_uint(rz * inv);
        r1[0] = __float_as_uint(sin_rev(d * fr[0]) * sc);
        r1[1] = __float_as_uint(sin_rev(d * fr[1]) * sc);
        r1[2] = __float_as_uint(sin_rev(d * fr[2]) * sc);
        r1[3] = __float_as_uint(sin_rev(d * fr[3]) * sc);
        u32x4* o = reinterpret_cast<u32x4*>(rec + (size_t)chunk * CH + pos);
        o[0] = r0;
        o[1] = r1;
    }
    STAMP(5);
}

__global__ __launch_bounds__(K1_THREADS) void painn_k1(const unsigned short* __restrict__ sv,
                                                       const unsigned char* __restrict__ posb,
                                                       const int* __restrict__ src, const int* __restrict__ dst,
                                                       const unsigned short* __restrict__ freqs,
                                                       const unsigned short* __restrict__ w1,
                                                       const unsigned short* __restrict__ w2,
                                                       unsigned char* __restrict__ TE, Rec* __restrict__ rec,
                                                       unsigned* __restrict__ tab, int N, int E, int BN, int NB,
                                                       int nchunks, int ngemm, unsigned binmagic) {
    extern __shared__ __attribute__((aligned(16))) char smem[];
    const int b = blockIdx.x;
    STAMPX(0);
    if (b < nchunks) {
        if (6 * N + 16 <= POS_LDS_MAX)
            edge_block<true>(b, nchunks, src, dst, posb, freqs, rec, tab, N, E, BN, NB, binmagic, smem);
        else
            edge_block<false>(b, nchunks, src, dst, posb, freqs, rec, tab, N, E, BN, NB, binmagic, smem);
    } else {
        gemm_block(b - nchunks, sv, w1, w2, TE, N, smem);
    }
}

constexpr int K2_NW = 16;
constexpr int K2_THREADS = K2_NW * 64;
constexpr int K2_BN = 400;
constexpr int K2_NG = K2_NW * 16;
constexpr int K2_LMAX = 10;
constexpr int K2_CAP = K2_NG * K2_LMAX;
constexpr int RMAX = K2_CAP / 512;
constexpr int K2_MAXCH = 256;
constexpr int O_STG = 0;
constexpr int O_ITEMS = K2_BN * 128;
constexpr int O_NOFF = O_ITEMS + (K2_CAP + 16) * 32;
constexpr int O_HEAD = O_NOFF + K2_BN * 4;
constexpr int O_LENT = O_HEAD + K2_NG * 4;
constexpr int O_WSUM = O_LENT + K2_MAXCH * 4;
constexpr int K2_SMEM = O_WSUM + 64;
static_assert(K2_SMEM <= 163840, "LDS");
static_assert(16 * K2_BN * 4 <= K2_BN * 128, "hist alias");
constexpr unsigned SRC_MASK = 0x3fffffu;

struct EData { u32x4 a, b, c; };

__global__ __launch_bounds__(K2_THREADS) void painn_k2(const unsigned char* __restrict__ TE,
                                                       const unsigned short* __restrict__ wrbf,
                                                       const Rec* __restrict__ rec, const unsigned* __restrict__ tab,
                                                       unsigned char* __restrict__ outb, int N, int BN, int NB, int nchunks) {
    extern __shared__ __attribute__((aligned(16))) char smem[];
    const int tid = threadIdx.x;
    const int lane = tid & 63;
    const int w = __builtin_amdgcn_readfirstlane(tid >> 6);
    const int x = blockIdx.x & 7;
    const int bin = blockIdx.x >> 3;
    STAMP2(0); STAMPX(2048);
    const int node0 = bin * BN;
    const int nn = min(BN, N - node0);
    char* stg = smem + O_STG;
    unsigned* hist = reinterpret_cast<unsigned*>(smem + O_STG);
    u32x4* items = reinterpret_cast<u32x4*>(smem + O_ITEMS);
    unsigned* idx = reinterpret_cast<unsigned*>(smem + O_ITEMS);
    unsigned* noff = reinterpret_cast<unsigned*>(smem + O_NOFF);
    unsigned* headinfo = reinterpret_cast<unsigned*>(smem + O_HEAD);
    unsigned* lent = reinterpret_cast<unsigned*>(smem + O_LENT);
    unsigned* wsum = reinterpret_cast<unsigned*>(smem + O_WSUM);

    const unsigned tsoff = (unsigned)N * 192u;
    const __amdgpu_buffer_rsrc_t rTE =
        __builtin_amdgcn_make_buffer_rsrc((void*)(TE + (size_t)x * N * 224), 0, (unsigned)N * 224u, 0x00020000);
    const __amdgpu_buffer_rsrc_t rOut =
        __builtin_amdgcn_make_buffer_rsrc((void*)outb, 0, (unsigned)N * 1024u, 0x00020000);

    if (tid < K2_MAXCH) lent[tid] = tid < nchunks ? tab[(size_t)bin * nchunks + tid] : 0u;
    u32x2 wpk[3][4];
    {
        const int cq = lane & 3;
#pragma unroll
        for (int sg = 0; sg < 3; ++sg)
#pragma unroll
            for (int k = 0; k < 4; ++k)
                wpk[sg][k] = *reinterpret_cast<const u32x2*>(wrbf + k * D3 + sg * D + 16 * x + 4 * cq);
    }
    for (int i = tid; i < 16 * K2_BN / 4; i += K2_THREADS) reinterpret_cast<u32x4*>(hist)[i] = u32x4{0u, 0u, 0u, 0u};
    if (tid < K2_NG) headinfo[tid] = 0xffffffffu;
    __syncthreads();
    int T;
    {
        const u32x4 e = reinterpret_cast<const u32x4*>(lent)[lane];
        const unsigned c0 = e[0] >> 16, c1 = e[1] >> 16, c2 = e[2] >> 16, c3 = e[3] >> 16;
        const unsigned incl = wave_incl_scan(c0 + c1 + c2 + c3, lane);
        T = __builtin_amdgcn_readlane((int)incl, 63);
        const unsigned o3 = incl - c3, o2 = o3 - c2, o1 = o2 - c1, o0 = o1 - c0;
        const unsigned me = (w & 3) == 0 ? e[0] : (w & 3) == 1 ? e[1] : (w & 3) == 2 ? e[2] : e[3];
        const unsigned mo = (w & 3) == 0 ? o0 : (w & 3) == 1 ? o1 : (w & 3) == 2 ? o2 : o3;
#pragma unroll 1
        for (int l = w >> 2; 4 * l + (w & 3) < nchunks; l += 4) {
            const unsigned ee = (unsigned)__builtin_amdgcn_readlane((int)me, l);
            const unsigned o = (unsigned)__builtin_amdgcn_readlane((int)mo, l);
            const unsigned cnt = ee >> 16, gb = (unsigned)(4 * l + (w & 3)) * CH + (ee & 0xffffu);
#pragma unroll 1
            for (unsigned i = (unsigned)lane; i < cnt; i += 64u)
                if (o + i < (unsigned)K2_CAP) idx[o + i] = gb + i;
        }
    }
    const int Tc = min(T, K2_CAP);
    const int L = max(1, (Tc + K2_NG - 1) / K2_NG);
    __syncthreads();
    if (tid == 0) STAMP2(1);
    const u32x4* rec4 = reinterpret_cast<const u32x4*>(rec);
    const int hp = tid >> 1, par = tid & 1;
    u32x4 rh[RMAX];
#pragma unroll
    for (int r = 0; r < RMAX; ++r) {
        const int i = 512 * r + hp;
        const unsigned id = (i < Tc) ? idx[i] : 0u;
        rh[r] = rec4[(size_t)id * 2 + par];
    }
    unsigned* myhist = hist + w * K2_BN;
    unsigned rank[RMAX];
#pragma unroll
    for (int r = 0; r < RMAX; ++r) {
        rank[r] = 0;
        if (512 * r + hp < Tc && par == 0) rank[r] = atomicAdd(&myhist[(rh[r][0] >> 22) & 511u], 1u);
    }
    __syncthreads();
    unsigned ntot = 0;
    if (tid < K2_BN) {
        unsigned h[16];
#pragma unroll
        for (int ww = 0; ww < 16; ++ww) h[ww] = hist[ww * K2_BN + tid];
#pragma unroll
        for (int ww = 0; ww < 16; ++ww) { hist[ww * K2_BN + tid] = ntot; ntot += h[ww]; }
    }
    const unsigned nincl = wave_incl_scan(ntot, lane);
    if (lane == 63) wsum[w] = nincl;
    __syncthreads();
    if (tid < K2_BN) {
        unsigned o = nincl - ntot;
#pragma unroll
        for (int ww = 0; ww < 7; ++ww) o += (ww < w) ? wsum[ww] : 0u;
        noff[tid] = o | (ntot << 16);
    }
    for (int i = 2 * Tc + tid; i < 2 * (K2_NG * L + 8); i += K2_THREADS) items[i] = u32x4{0u, 0u, 0u, 0u};
    __syncthreads();
    const unsigned Mdiv = 0xffffffffu / (unsigned)L + 1u;
#pragma unroll
    for (int r = 0; r < RMAX; ++r) {
        if (512 * r < Tc) {
            const bool valid = 512 * r + hp < Tc;
            u32x4 t = rh[r];
            unsigned p = 0;
            if (valid && par == 0) {
                const unsigned dl = (t[0] >> 22) & 511u;
                const unsigned no = noff[dl];
                const unsigned rk = myhist[dl] + rank[r];
                p = (no & 0xffffu) + rk;
                if (rk + 1u == (no >> 16)) {
                    t[0] |= 0x80000000u;
                    const unsigned gg = L == 1 ? p : __umulhi(p, Mdiv), g0 = L == 1 ? (no & 0xffffu) : __umulhi(no & 0xffffu, Mdiv);
                    if (g0 < gg) headinfo[gg] = dl | (g0 << 16);
                }
            }
            p = (unsigned)__builtin_amdgcn_update_dpp(0, (int)p, 0xA0, 0xf, 0xf, false);
            if (valid) items[2 * p + par] = t;
        }
    }
    __syncthreads();
    for (int i = tid; i < K2_BN * 8; i += K2_THREADS) reinterpret_cast<u32x4*>(stg)[i] = u32x4{0u, 0u, 0u, 0u};
    float wr[3][4][4];
#pragma unroll
    for (int sg = 0; sg < 3; ++sg)
#pragma unroll
        for (int k = 0; k < 4; ++k) {
            asm volatile("" : "+v"(wpk[sg][k]));
            wr[sg][k][0] = bf_lo(wpk[sg][k][0]); wr[sg][k][1] = bf_hi(wpk[sg][k][0]);
            wr[sg][k][2] = bf_lo(wpk[sg][k][1]); wr[sg][k][3] = bf_hi(wpk[sg][k][1]);
        }
    __syncthreads();
    if (tid == 0) STAMP2(2);

    float acc[16];
#pragma unroll
    for (int r = 0; r < 16; ++r) acc[r] = 0.f;
    const int cq = lane & 3;
    const u32x4* ipA = items + 2 * ((tid >> 2) * L);

    auto issue = [&](EData& d, unsigned key) LAM {
        const unsigned vo = (key & SRC_MASK) * 192u + (unsigned)cq * 16u;
        d.a = __builtin_amdgcn_raw_buffer_load_b128(rTE, vo, 0, 0);
        d.b = __builtin_amdgcn_raw_buffer_load_b128(rTE, vo + 64u, 0, 0);
        d.c = __builtin_amdgcn_raw_buffer_load_b128(rTE, vo + 128u, 0, 0);
    };
    auto compute = [&](EData& d, const u32x4 rr, const u32x4 kk) LAM {
        const float hx = __uint_as_float(rr[1]), hy = __uint_as_float(rr[2]), hz = __uint_as_float(rr[3]);
        const float c0 = __uint_as_float(kk[0]), c1 = __uint_as_float(kk[1]), c2 = __uint_as_float(kk[2]), c3 = __uint_as_float(kk[3]);
#pragma unroll
        for (int j = 0; j < 4; ++j) {
            float wfs = mul_s(c0, wr[0][0][j]), wfv = mul_s(c0, wr[1][0][j]), wfr = mul_s(c0, wr[2][0][j]);
            fmac_s(wfs, c1, wr[0][1][j]); fmac_s(wfv, c1, wr[1][1][j]); fmac_s(wfr, c1, wr[2][1][j]);
            fmac_s(wfs, c2, wr[0][2][j]); fmac_s(wfv, c2, wr[1][2][j]); fmac_s(wfr, c2, wr[2][2][j]);
            fmac_s(wfs, c3, wr[0][3][j]); fmac_s(wfv, c3, wr[1][3][j]); fmac_s(wfr, c3, wr[2][3][j]);
            const float ux = bf_lo(d.a[j]), uy = bf_lo(d.b[j]), uz = bf_lo(d.c[j]);
            const float mv = mul_s(__uint_as_float(d.b[j]), wfv);
            const float mr = mul_s(__uint_as_float(d.c[j]), wfr);
            fmac_s(acc[j], __uint_as_float(d.a[j]), wfs);
            fmac_s(acc[4 + j], mv, ux); fmac_s(acc[8 + j], mv, uy); fmac_s(acc[12 + j], mv, uz);
            fmac_s(acc[4 + j], mr, hx); fmac_s(acc[8 + j], mr, hy); fmac_s(acc[12 + j], mr, hz);
        }
        const bool last = (int)rr[0] < 0;
        if (last) {
            char* sp = stg + ((rr[0] >> 22) & 511u) * 128u + cq * 8;
#pragma unroll
            for (int r = 0; r < 4; ++r) {
                u32x2 v;
                v[0] = pack_bf16(acc[4 * r + 0], acc[4 * r + 1]);
                v[1] = pack_bf16(acc[4 * r + 2], acc[4 * r + 3]);
                *reinterpret_cast<u32x2*>(sp + r * 32) = v;
            }
        }
        masked_zero16(acc, __ballot(last));
    };

    {
        EData dA, dB;
#define SB __builtin_amdgcn_sched_barrier(0)
        u32x4 ra = ipA[0], ka = ipA[1], rb = ipA[2], kb = ipA[3];
        SB; issue(dA, ra[0]);
        SB; issue(dB, rb[0]);
        unsigned k2 = ipA[4][0], k3 = ipA[6][0];
        SB;
        auto body = [&](int it) LAM {
            const u32x4 ra2 = ipA[2 * (it + 2)], ka2 = ipA[2 * (it + 2) + 1];
            compute(dA, ra, ka);
            SB; issue(dA, k2); SB;
            ra = ra2; ka = ka2;
            const u32x4 rb2 = ipA[2 * (it + 3)], kb2 = ipA[2 * (it + 3) + 1];
            k2 = ipA[2 * (it + 4)][0];
            compute(dB, rb, kb);
            SB; issue(dB, k3); SB;
            rb = rb2; kb = kb2;
            k3 = ipA[2 * (it + 5)][0];
        };
        int it = 0;
        if (L >= 2) {
            body(0);
            for (it = 2; it + 1 < L; it += 2) body(it);
        }
        if (it < L) compute(dA, ra, ka);
    }
    if (tid == 0) STAMP2(3);
    __syncthreads();

    int tid2;
    asm volatile("v_mbcnt_lo_u32_b32 %0, -1, 0\n\tv_mbcnt_hi_u32_b32 %0, -1, %0" : "=v"(tid2));
    tid2 += w * 64;
    u32x4 ev[5], es;
#pragma unroll
    for (int q = 0; q < 5; ++q) {
        const int c = K2_THREADS * q + tid2;
        ev[q] = __builtin_amdgcn_raw_buffer_load_b128(rTE, c < nn * 12 ? (unsigned)node0 * 192u + (unsigned)c * 16u : 0x80000000u, 0, 0);
    }
    es = __builtin_amdgcn_raw_buffer_load_b128(rTE, tid2 < nn * 2 ? tsoff + (unsigned)node0 * 32u + (unsigned)tid2 * 16u : 0x80000000u, 0, 0);
    {
        f32x4* tl = reinterpret_cast<f32x4*>(items);
        const int G = tid2 >> 2, cq2 = tid2 & 3;
#pragma unroll
        for (int r = 0; r < 4; ++r) tl[(G * 4 + r) * 4 + cq2] = f32x4{acc[4 * r], acc[4 * r + 1], acc[4 * r + 2], acc[4 * r + 3]};
        __syncthreads();
        const unsigned hi = headinfo[G];
        if (hi != 0xffffffffu) {
            const int g0 = (int)(hi >> 16);
            f32x4 t[4];
#pragma unroll
            for (int r = 0; r < 4; ++r) t[r] = tl[(g0 * 4 + r) * 4 + cq2];
#pragma unroll 1
            for (int gg = g0 + 1; gg < G; ++gg) {
#pragma unroll
                for (int r = 0; r < 4; ++r) t[r] += tl[(gg * 4 + r) * 4 + cq2];
            }
            char* sp = stg + (hi & 511u) * 128u + cq2 * 8;
#pragma unroll
            for (int r = 0; r < 4; ++r) {
                const u32x2 o = *reinterpret_cast<const u32x2*>(sp + r * 32);
                u32x2 v;
                v[0] = pack_bf16(t[r][0] + bf_lo(o[0]), t[r][1] + bf_hi(o[0]));
                v[1] = pack_bf16(t[r][2] + bf_lo(o[1]), t[r][3] + bf_hi(o[1]));
                *reinterpret_cast<u32x2*>(sp + r * 32) = v;
            }
        }
        __syncthreads();
    }
#pragma unroll
    for (int q = 0; q < 5; ++q) {
        const unsigned c = (unsigned)(K2_THREADS * q + tid2);
        const unsigned j = __umulhi(c, 357913942u);
        const unsigned cc = c - j * 12u, r = (cc >> 2) + 1u, cq2 = cc & 3u;
        const u32x2 dl = *reinterpret_cast<const u32x2*>(stg + min(j, (unsigned)(K2_BN - 1)) * 128u + r * 32u + cq2 * 8u);
        u32x2 o;
        o[0] = pack_bf16(bf_lo(ev[q][0]) + bf_lo(dl[0]), bf_lo(ev[q][1]) + bf_hi(dl[0]));
        o[1] = pack_bf16(bf_lo(ev[q][2]) + bf_lo(dl[1]), bf_lo(ev[q][3]) + bf_hi(dl[1]));
        const unsigned oo = (int)j < nn ? ((unsigned)node0 + j) * 1024u + r * 256u + (unsigned)x * 32u + cq2 * 8u : 0x80000000u;
        __builtin_amdgcn_raw_buffer_store_b64(o, rOut, oo, 0, 0);
    }
    {
        const unsigned j = (unsigned)tid2 >> 1, h = (unsigned)tid2 & 1u;
        const u32x4 dl = *reinterpret_cast<const u32x4*>(stg + min(j, (unsigned)(K2_BN - 1)) * 128u + h * 16u);
        u32x4 o;
#pragma unroll
        for (int k = 0; k < 4; ++k) o[k] = pack_bf16(bf_lo(es[k]) + bf_lo(dl[k]), bf_hi(es[k]) + bf_hi(dl[k]));
        const unsigned oo = (int)j < nn ? ((unsigned)node0 + j) * 1024u + (unsigned)x * 32u + h * 16u : 0x80000000u;
        __builtin_amdgcn_raw_buffer_store_b128(o, rOut, oo, 0, 0);
    }
    STAMPW16();
}

extern "C" void kernel_launch(void* const* d_in, const int* in_sizes, int n_in, void* d_out, int out_size,
                              void* d_ws, size_t ws_size, hipStream_t stream) {
    const unsigned short* sv = (const unsigned short*)d_in[0];
    const unsigned char* pos = (const unsigned char*)d_in[1];
    const int* src = (const int*)d_in[2];
    const int* dst = (const int*)d_in[3];
    const unsigned short* freqs = (const unsigned short*)d_in[4];
    const unsigned short* w1 = (const unsigned short*)d_in[5];
    const unsigned short* w2 = (const unsigned short*)d_in[6];
    const unsigned short* wrbf = (const unsigned short*)d_in[7];
    const int N = in_sizes[0] / (4 * D);
    const int E = in_sizes[2];

    int BN = (N + 31) / 32;
    if (BN > 400) BN = 400;
    const int NB = (N + BN - 1) / BN;
    const int nchunks = (E + CH - 1) / CH;
    const int ngemm = (N + G_ROWS - 1) / G_ROWS;
    const unsigned binmagic = (unsigned)(0xffffffffull / (unsigned)BN + 1ull);

    char* ws = (char*)d_ws;
    unsigned char* TE = (unsigned char*)ws;
    size_t off = ((size_t)N * 8 * 224 + 255) & ~(size_t)255;
    Rec* rec = (Rec*)(ws + off);
    off += (size_t)nchunks * CH * sizeof(Rec);
    unsigned* tab = (unsigned*)(ws + off);

    (void)hipFuncSetAttribute((const void*)painn_k1, hipFuncAttributeMaxDynamicSharedMemorySize, K1_SMEM);
    painn_k1<<<ngemm + nchunks, K1_THREADS, K1_SMEM, stream>>>(sv, pos, src, dst, freqs, w1, w2, TE, rec, tab, N, E, BN,
                                                                NB, nchunks, ngemm, binmagic);
    (void)hipFuncSetAttribute((const void*)painn_k2, hipFuncAttributeMaxDynamicSharedMemorySize, K2_SMEM);
    painn_k2<<<NB * 8, K2_THREADS, K2_SMEM, stream>>>(TE, wrbf, rec, tab, (unsigned char*)d_out, N, BN, NB, nchunks);
}
```

```cpp
#include <hip/hip_runtime.h>
#include <hip/hip_bf16.h>
#include <stdint.h>

using bf16x8 = __attribute__((ext_vector_type(8))) short;
using f32x4 = __attribute__((ext_vector_type(4))) float;
using u32x4 = __attribute__((ext_vector_type(4))) unsigned;
using u32x2 = __attribute__((ext_vector_type(2))) unsigned;
using i32x4 = __attribute__((ext_vector_type(4))) int;
typedef __bf16 bf16x2_t __attribute__((ext_vector_type(2)));
typedef float f32x2 __attribute__((ext_vector_type(2)));

#define DI __device__ __forceinline__
#define LAM __attribute__((always_inline))
DI float fma_s(float a, float b, float c) { float d; asm("v_fma_f32 %0, %1, %2, %3" : "=v"(d) : "v"(a), "v"(b), "v"(c)); return d; }
DI float mul_s(float a, float b) { float d; asm("v_mul_f32 %0, %1, %2" : "=v"(d) : "v"(a), "v"(b)); return d; }
DI void masked_copy8(float* d, const float* s, unsigned long long mask) {
    unsigned long long sv;
    asm volatile("s_and_saveexec_b64 %8, %17\n\ts_cbranch_execz .Lmc%=\n\t"
                 "v_mov_b32 %0, %9\n\tv_mov_b32 %1, %10\n\tv_mov_b32 %2, %11\n\tv_mov_b32 %3, %12\n\t"
                 "v_mov_b32 %4, %13\n\tv_mov_b32 %5, %14\n\tv_mov_b32 %6, %15\n\tv_mov_b32 %7, %16\n"
                 ".Lmc%=:\n\ts_mov_b64 exec, %8"
                 : "+v"(d[0]), "+v"(d[1]), "+v"(d[2]), "+v"(d[3]), "+v"(d[4]), "+v"(d[5]), "+v"(d[6]), "+v"(d[7]), "=&s"(sv)
                 : "v"(s[0]), "v"(s[1]), "v"(s[2]), "v"(s[3]), "v"(s[4]), "v"(s[5]), "v"(s[6]), "v"(s[7]), "s"(mask)
                 : "scc");
}
DI void masked_zero16(float* d, unsigned long long mask) {
    unsigned long long sv;
    asm volatile("s_and_saveexec_b64 %16, %17\n\ts_cbranch_execz .Lmz%=\n\t"
                 "v_mov_b32 %0, 0\n\tv_mov_b32 %1, 0\n\tv_mov_b32 %2, 0\n\tv_mov_b32 %3, 0\n\t"
                 "v_mov_b32 %4, 0\n\tv_mov_b32 %5, 0\n\tv_mov_b32 %6, 0\n\tv_mov_b32 %7, 0\n\t"
                 "v_mov_b32 %8, 0\n\tv_mov_b32 %9, 0\n\tv_mov_b32 %10, 0\n\tv_mov_b32 %11, 0\n\t"
                 "v_mov_b32 %12, 0\n\tv_mov_b32 %13, 0\n\tv_mov_b32 %14, 0\n\tv_mov_b32 %15, 0\n"
                 ".Lmz%=:\n\ts_mov_b64 exec, %16"
                 : "+v"(d[0]), "+v"(d[1]), "+v"(d[2]), "+v"(d[3]), "+v"(d[4]), "+v"(d[5]), "+v"(d[6]), "+v"(d[7]),
                   "+v"(d[8]), "+v"(d[9]), "+v"(d[10]), "+v"(d[11]), "+v"(d[12]), "+v"(d[13]), "+v"(d[14]), "+v"(d[15]), "=&s"(sv)
                 : "s"(mask) : "scc");
}
DI void fmac_s(float& d, float a, float b) { asm("v_fmac_f32 %0, %1, %2" : "+v"(d) : "v"(a), "v"(b)); }
#define STAMP(i)
#define STAMP2(i)
#define STAMPW(i)
#define STAMPW16()
#define STAMPX(i)

constexpr int D = 128;
constexpr int D3 = 384;

DI float bf_lo(unsigned u) { return __uint_as_float(u << 16); }
DI float bf_hi(unsigned u) { return __uint_as_float(u & 0xffff0000u); }
DI unsigned pack_bf16(float lo, float hi) {
    f32x2 v = {lo, hi};
    bf16x2_t b = __builtin_convertvector(v, bf16x2_t);
    return __builtin_bit_cast(unsigned, b);
}
DI float silu_f(float x) { return x * __builtin_amdgcn_rcpf(1.0f + __expf(-x)); }

struct Rec {
    unsigned key;
    float c[4];
    float rh[3];
};

typedef __attribute__((address_space(3))) void* lds_ptr_t;
typedef short s16x4 __attribute__((ext_vector_type(4)));
typedef __attribute__((address_space(3))) s16x4* lds_s16x4_ptr;

DI unsigned wave_incl_scan(unsigned v, int lane) {
    v += (unsigned)__builtin_amdgcn_update_dpp(0, (int)v, 0x111, 0xf, 0xf, false);
    v += (unsigned)__builtin_amdgcn_update_dpp(0, (int)v, 0x112, 0xf, 0xf, false);
    v += (unsigned)__builtin_amdgcn_update_dpp(0, (int)v, 0x114, 0xf, 0xf, false);
    v += (unsigned)__builtin_amdgcn_update_dpp(0, (int)v, 0x118, 0xf, 0xf, false);
    v += (unsigned)__builtin_amdgcn_update_dpp(0, (int)v, 0x142, 0xa, 0xf, false);
    v += (unsigned)__builtin_amdgcn_update_dpp(0, (int)v, 0x143, 0xc, 0xf, false);
    return v;
}
DI unsigned mbcnt64(unsigned long long m) {
    return __builtin_amdgcn_mbcnt_hi((unsigned)(m >> 32), __builtin_amdgcn_mbcnt_lo((unsigned)m, 0));
}
DI void dma16(const __amdgpu_buffer_rsrc_t rsrc, unsigned voff, unsigned lds_dst) {
    unsigned keep;
    asm volatile("s_mov_b32 %0, m0\n\ts_mov_b32 m0, %3\n\ts_nop 0\n\tbuffer_load_dwordx4 %1, %2, 0 offen lds\n\ts_mov_b32 m0, %0"
                 : "=&s"(keep) : "v"(voff), "s"(rsrc), "s"(lds_dst) : "memory");
}
DI unsigned lds_addr(const void* p) { return (unsigned)(size_t)(lds_ptr_t)p; }

constexpr int K1_THREADS = 512;
constexpr int G_TILES = 5;
constexpr int G_ROWS = 16 * G_TILES;
constexpr int HP = 272;
constexpr int GO_S = 131072;
constexpr int GO_H = 0;
constexpr int GO_OST = 32768;
constexpr int OP = 1040;
constexpr int K1_SMEM = 163840;
#define ST_AUX 16

DI unsigned wswz(unsigned row) { return ((row & 3u) << 2) | ((row >> 2) & 3u); }

DI void gemm_block(int blk, const unsigned short* __restrict__ sv, const unsigned short* __restrict__ w1,
                   const unsigned short* __restrict__ w2, unsigned char* __restrict__ TP, int N, char* smem) {
    const int tid = threadIdx.x;
    const int lane = tid & 63;
    const int w = __builtin_amdgcn_readfirstlane(tid >> 6);
    const int i16 = lane & 15, g = lane >> 4;
    const int row0 = blk * G_ROWS;
    const unsigned sm = lds_addr(smem);
    STAMP(0);
    const __amdgpu_buffer_rsrc_t rW1 = __builtin_amdgcn_make_buffer_rsrc((void*)w1, 0, D * D * 2, 0x00020000);
    const __amdgpu_buffer_rsrc_t rW2 = __builtin_amdgcn_make_buffer_rsrc((void*)w2, 0, D * D3 * 2, 0x00020000);
    const __amdgpu_buffer_rsrc_t rSV = __builtin_amdgcn_make_buffer_rsrc((void*)sv, 0, (unsigned)N * 1024u, 0x00020000);
    const unsigned rot = (unsigned)blk * 5u;

#pragma unroll
    for (int q = 0; q < 4; ++q) {
        const unsigned i = ((unsigned)(w + 8 * q) + rot) & 31u;
        const unsigned row = 4u * i + (unsigned)g;
        dma16(rW1, row * 256u + (((unsigned)i16 ^ wswz(row)) * 16u), sm + i * 1024u);
    }
#pragma unroll
    for (int q = 0; q < 3; ++q) {
        const unsigned i = (unsigned)(w + 8 * q);
        if (i < G_ROWS / 4) {
            const unsigned node = 4u * i + (unsigned)g;
            dma16(rSV, ((unsigned)row0 + node) * 1024u + (((unsigned)i16 ^ (node & 15u)) * 16u), sm + GO_S + i * 1024u);
        }
    }
#pragma unroll
    for (int q = 0; q < 12; ++q) {
        const unsigned i = ((unsigned)(w + 8 * (q & 3)) + rot) & 31u, seg = (unsigned)(q >> 2);
        const unsigned row = 4u * i + (unsigned)g;
        dma16(rW2, row * 768u + seg * 256u + (((unsigned)i16 ^ wswz(row)) * 16u), sm + 32768u * (seg + 1u) + i * 1024u);
    }
    asm volatile("s_waitcnt vmcnt(12)" ::: "memory");
    __syncthreads();
    STAMP(1);
    const unsigned tq = (unsigned)i16 >> 2, tp = (unsigned)lane & 3u;
    auto afrag = [&](int im, int ks) LAM {
        const unsigned row = 32u * ks + 8u * g + tq;
        const s16x4 lo = __builtin_amdgcn_ds_read_tr16_b64_v4i16((lds_s16x4_ptr)(smem + im * 32768 + 256u * row +
                            16u * ((2u * w + (tp >> 1)) ^ wswz(row)) + 8u * (tp & 1u)));
        const s16x4 hi = __builtin_amdgcn_ds_read_tr16_b64_v4i16((lds_s16x4_ptr)(smem + im * 32768 + 256u * (row + 4u) +
                            16u * ((2u * w + (tp >> 1)) ^ wswz(row + 4u)) + 8u * (tp & 1u)));
        return bf16x8{lo[0], lo[1], lo[2], lo[3], hi[0], hi[1], hi[2], hi[3]};
    };
    bf16x8 a1[4];
#pragma unroll
    for (int ks = 0; ks < 4; ++ks) a1[ks] = afrag(0, ks);
    __syncthreads();
#pragma unroll 1
    for (int t = 0; t < G_TILES; ++t) {
        const char* st = smem + GO_S + (16 * t + i16) * 256;
        f32x4 c = {0.f, 0.f, 0.f, 0.f};
#pragma unroll
        for (int ks = 0; ks < 4; ++ks) {
            const bf16x8 b = *reinterpret_cast<const bf16x8*>(st + (((unsigned)(4 * ks + g) ^ (unsigned)i16) * 16u));
            c = __builtin_amdgcn_mfma_f32_16x16x32_bf16(a1[ks], b, c, 0, 0, 0);
        }
        u32x2 o;
        o[0] = pack_bf16(silu_f(c[0]), silu_f(c[1]));
        o[1] = pack_bf16(silu_f(c[2]), silu_f(c[3]));
        *reinterpret_cast<u32x2*>(smem + GO_H + (16 * t + i16) * HP + (16 * w + 4 * g) * 2) = o;
    }
    STAMP(2);
    asm volatile("s_waitcnt vmcnt(0)" ::: "memory");
    __syncthreads();
    bf16x8 a2[3][4];
#pragma unroll
    for (int q = 0; q < 3; ++q)
#pragma unroll
        for (int ks = 0; ks < 4; ++ks) a2[q][ks] = afrag(q + 1, ks);
    __syncthreads();
    STAMP(3);
    const __amdgpu_buffer_rsrc_t rT = __builtin_amdgcn_make_buffer_rsrc((void*)TP, 0, (unsigned)N * 1024u, 0x00020000);
    const unsigned sw = ((((unsigned)(2 * w + (g >> 1))) ^ (unsigned)i16) * 16u) + (unsigned)(g & 1) * 8u;
#pragma unroll 1
    for (int t = 0; t < G_TILES; ++t) {
        char* ost = smem + GO_OST + (t & 1) * (16 * OP);
        bf16x8 bh[4];
#pragma unroll
        for (int ks = 0; ks < 4; ++ks)
            bh[ks] = *reinterpret_cast<const bf16x8*>(smem + GO_H + (16 * t + i16) * HP + (32 * ks + 8 * g) * 2);
        *reinterpret_cast<u32x2*>(ost + i16 * OP + 768 + 32 * w + 8 * g) =
            *reinterpret_cast<const u32x2*>(smem + GO_S + (16 * t + i16) * 256 + sw);
#pragma unroll
        for (int q = 0; q < 3; ++q) {
            f32x4 c = {0.f, 0.f, 0.f, 0.f};
#pragma unroll
            for (int ks = 0; ks < 4; ++ks) c = __builtin_amdgcn_mfma_f32_16x16x32_bf16(a2[q][ks], bh[ks], c, 0, 0, 0);
            u32x2 o;
            o[0] = pack_bf16(c[0], c[1]);
            o[1] = pack_bf16(c[2], c[3]);
            *reinterpret_cast<u32x2*>(ost + i16 * OP + q * 256 + 32 * w + 8 * g) = o;
        }
        __syncthreads();
#pragma unroll
        for (int q = 0; q < 2; ++q) {
            const int r = 2 * w + q;
            const u32x4 o = *reinterpret_cast<const u32x4*>(ost + r * OP + lane * 16);
            __builtin_amdgcn_raw_buffer_store_b128(o, rT, (unsigned)(row0 + 16 * t + r) * 1024u + (unsigned)lane * 16u, 0, ST_AUX);
        }
        if (t == 0) STAMP(4);
    }
    STAMP(5);
}

constexpr int EPT = 8;
constexpr int CH = K1_THREADS * EPT;
constexpr int MAXNB = 128;
constexpr int EO_POS = 0;
constexpr int POS_LDS_MAX = 98304;
constexpr int EO_SRC = POS_LDS_MAX;
constexpr int EO_DST = EO_SRC + CH * 4;
constexpr int EO_RANK = EO_DST + CH * 4;
constexpr int EO_HIST = EO_RANK + CH * 2;
static_assert(EO_HIST + 8 * MAXNB * 4 <= K1_SMEM, "LDS");

struct Pos3 { float x, y, z; };
DI Pos3 unpack_pos(unsigned a, unsigned b, unsigned odd16) {
    const unsigned lo = __builtin_amdgcn_alignbit(b, a, odd16);
    const unsigned hi = b >> odd16;
    return Pos3{bf_lo(lo), bf_hi(lo), bf_lo(hi)};
}
DI Pos3 pos_lds(const char* lp, unsigned n) {
    const unsigned off = 6u * n;
    const unsigned* p = reinterpret_cast<const unsigned*>(lp + (off & ~3u));
    return unpack_pos(p[0], p[1], (off & 2u) << 3);
}
DI Pos3 pos_glb(const unsigned char* __restrict__ posb, unsigned n, unsigned lim) {
    const unsigned off = 6u * n;
    const unsigned a = off < lim ? off : lim;
    unsigned long long v;
    __builtin_memcpy(&v, posb + a, 8);
    v >>= (off - a) * 8u;
    return Pos3{bf_lo((unsigned)v), bf_hi((unsigned)v), bf_lo((unsigned)(v >> 32))};
}
DI float sin_rev(float r) { return __builtin_amdgcn_sinf(r - __builtin_rintf(r)); }
DI float cos_rev(float r) { return __builtin_amdgcn_cosf(r - __builtin_rintf(r)); }

template <bool POS_LDS>
DI void edge_block(int chunk, int nchunks, const int* __restrict__ src, const int* __restrict__ dst,
                   const unsigned char* __restrict__ posb, const unsigned short* __restrict__ freqs,
                   Rec* __restrict__ rec, unsigned* __restrict__ tab, int N, int E,
                   int BN, int NB, unsigned binmagic, char* smem) {
    const int tid = threadIdx.x;
    const int lane = tid & 63;
    const int w = __builtin_amdgcn_readfirstlane(tid >> 6);
    unsigned* it_src = reinterpret_cast<unsigned*>(smem + EO_SRC);
    unsigned* it_dst = reinterpret_cast<unsigned*>(smem + EO_DST);
    unsigned short* it_rank = reinterpret_cast<unsigned short*>(smem + EO_RANK);
    unsigned* hist = reinterpret_cast<unsigned*>(smem + EO_HIST);
    const unsigned lim = 6u * (unsigned)N - 8u;
    STAMP(0);
    const int wbase = chunk * CH + w * (EPT * 64);
    int es[EPT], ed[EPT];
#pragma unroll
    for (int j = 0; j < EPT / 4; ++j) {
        const int e = wbase + (j * 64 + lane) * 4;
        if (e + 3 < E) {
            const i32x4 s4 = *reinterpret_cast<const i32x4*>(src + e);
            const i32x4 d4 = *reinterpret_cast<const i32x4*>(dst + e);
#pragma unroll
            for (int q = 0; q < 4; ++q) { es[4 * j + q] = s4[q]; ed[4 * j + q] = d4[q]; }
        } else {
#pragma unroll
            for (int q = 0; q < 4; ++q) {
                es[4 * j + q] = (e + q < E) ? src[e + q] : -1;
                ed[4 * j + q] = (e + q < E) ? dst[e + q] : 0;
            }
        }
    }
    if (POS_LDS) {
        const unsigned bytes = 6u * (unsigned)N;
        const __amdgpu_buffer_rsrc_t rP = __builtin_amdgcn_make_buffer_rsrc((void*)posb, 0, bytes & ~3u, 0x00020000);
        const unsigned sm = lds_addr(smem);
        for (unsigned o = (unsigned)w * 1024u; o < bytes; o += 8192u) dma16(rP, o + (unsigned)lane * 16u, sm + EO_POS + o);
    }
    hist[tid] = 0; hist[tid + K1_THREADS] = 0;
    if (POS_LDS) {
        asm volatile("s_waitcnt vmcnt(0)" ::: "memory");
        __syncthreads();
        if ((N & 1) && tid == 0)
            *reinterpret_cast<unsigned*>(smem + EO_POS + ((6u * (unsigned)N) & ~3u)) =
                (unsigned)*reinterpret_cast<const unsigned short*>(posb + 6u * (unsigned)N - 2u);
        if (N & 1) __syncthreads();
    } else {
        __syncthreads();
    }
    STAMP(1);
    int cnt = 0;
#pragma unroll
    for (int j = 0; j < EPT; ++j) {
        const unsigned s = (unsigned)(es[j] < 0 ? 0 : es[j]), d = (unsigned)ed[j];
        const Pos3 a = POS_LDS ? pos_lds(smem + EO_POS, s) : pos_glb(posb, s, lim);
        const Pos3 b = POS_LDS ? pos_lds(smem + EO_POS, d) : pos_glb(posb, d, lim);
        const float rx = a.x - b.x, ry = a.y - b.y, rz = a.z - b.z;
        const float d2 = rx * rx + ry * ry + rz * rz + 1e-8f;
        const bool act = (es[j] >= 0) && !(d2 >= 25.0f && d2 < __builtin_inff());
        const unsigned long long m = __ballot(act);
        if (act) {
            const int p = w * (EPT * 64) + cnt + (int)mbcnt64(m);
            it_src[p] = s;
            it_dst[p] = d;
        }
        cnt += __popcll(m);
    }
    STAMP(2);
    unsigned* myhist = hist + w * MAXNB;
#pragma unroll 1
    for (int b = lane; b < cnt; b += 64) {
        const int p = w * (EPT * 64) + b;
        it_rank[p] = (unsigned short)atomicAdd(&myhist[__umulhi(it_dst[p], binmagic)], 1u);
    }
    __syncthreads();
    if (w == 0) {
        u32x2 h[8];
        unsigned t0 = 0, t1 = 0;
#pragma unroll
        for (int ww = 0; ww < 8; ++ww) {
            h[ww] = *reinterpret_cast<const u32x2*>(hist + ww * MAXNB + 2 * lane);
            t0 += h[ww][0]; t1 += h[ww][1];
        }
        unsigned r0 = wave_incl_scan(t0 + t1, lane) - t0 - t1, r1 = r0 + t0;
        const int ncp = (nchunks + 3) & ~3;
        if (2 * lane < NB) tab[(size_t)(2 * lane) * ncp + chunk] = r0 | (t0 << 16);
        if (2 * lane + 1 < NB) tab[(size_t)(2 * lane + 1) * ncp + chunk] = r1 | (t1 << 16);
#pragma unroll
        for (int ww = 0; ww < 8; ++ww) {
            *reinterpret_cast<u32x2*>(hist + ww * MAXNB + 2 * lane) = u32x2{r0, r1};
            r0 += h[ww][0]; r1 += h[ww][1];
        }
    }
    __syncthreads();
    STAMP(3);
    float fr[4];
#pragma unroll
    for (int k = 0; k < 4; ++k) fr[k] = bf_lo((unsigned)freqs[k]) * 0.15915494309189535f;
#pragma unroll 1
    for (int b = lane; b < cnt; b += 64) {
        const int p = w * (EPT * 64) + b;
        const unsigned s = it_src[p], dd = it_dst[p];
        const Pos3 a = POS_LDS ? pos_lds(smem + EO_POS, s) : pos_glb(posb, s, lim);
        const Pos3 c = POS_LDS ? pos_lds(smem + EO_POS, dd) : pos_glb(posb, dd, lim);
        const float rx = a.x - c.x, ry = a.y - c.y, rz = a.z - c.z;
        const unsigned bin = __umulhi(dd, binmagic);
        const unsigned dl = dd - bin * (unsigned)BN;
        const unsigned pos = myhist[bin] + (unsigned)it_rank[p];
        const float d2 = rx * rx + ry * ry + rz * rz + 1e-8f;
        const float d = __builtin_sqrtf(d2);
        const float inv = 1.0f / d;
        float fc = 0.5f * (cos_rev(d * 0.1f) + 1.0f);
        fc = (d < 5.0f) ? fc : 0.0f * d;
        const float sc = inv * fc;
        u32x4 r0, r1;
        r0[0] = s | (dl << 22);
        r0[1] = __float_as_uint(rx * inv);
        r0[2] = __float_as_uint(ry * inv);
        r0[3] = __float_as_uint(rz * inv);
        r1[0] = __float_as_uint(sin_rev(d * fr[0]) * sc);
        r1[1] = __float_as_uint(sin_rev(d * fr[1]) * sc);
        r1[2] = __float_as_uint(sin_rev(d * fr[2]) * sc);
        r1[3] = __float_as_uint(sin_rev(d * fr[3]) * sc);
        u32x4* o = reinterpret_cast<u32x4*>(rec + (size_t)chunk * CH + pos);
        o[0] = r0;
        o[1] = r1;
    }
    STAMP(5);
}

__global__ __launch_bounds__(K1_THREADS) void painn_k1(const unsigned short* __restrict__ sv,
                                                       const unsigned char* __restrict__ posb,
                                                       const int* __restrict__ src, const int* __restrict__ dst,
                                                       const unsigned short* __restrict__ freqs,
                                                       const unsigned short* __restrict__ w1,
                                                       const unsigned short* __restrict__ w2,
                                                       unsigned char* __restrict__ TP, Rec* __restrict__ rec,
                                                       unsigned* __restrict__ tab, int N, int E, int BN, int NB,
                                                       int nchunks, int ngemm, unsigned binmagic) {
    extern __shared__ __attribute__((aligned(16))) char smem[];
    const int b = blockIdx.x;
    STAMPX(0);
    if (b < nchunks) {
        if (6 * N + 16 <= POS_LDS_MAX)
            edge_block<true>(b, nchunks, src, dst, posb, freqs, rec, tab, N, E, BN, NB, binmagic, smem);
        else
            edge_block<false>(b, nchunks, src, dst, posb, freqs, rec, tab, N, E, BN, NB, binmagic, smem);
    } else {
        gemm_block(b - nchunks, sv, w1, w2, TP, N, smem);
    }
}

constexpr int K2_NW = 16;
constexpr int K2_THREADS = K2_NW * 64;
constexpr int K2_BN = 100;
constexpr int K2_NG = K2_NW * 2;
constexpr int K2_CAP = 768;
constexpr int K2_ICAP = K2_CAP + K2_BN + K2_NG + 20;
constexpr int K2_MAXCH = 256;
constexpr int K2_R = 5;
constexpr int K2_SLOT = 1536;
constexpr int O_RING = 0;
constexpr int O_ITEMS = K2_NW * K2_R * K2_SLOT;
constexpr int O_PERM = O_ITEMS + (K2_CAP + 1) * 32;
constexpr int O_SKEY = O_PERM + K2_ICAP * 2;
constexpr int O_NOFF = O_SKEY + K2_ICAP * 4;
constexpr int O_HEAD = O_NOFF + 128 * 4;
constexpr int O_WSUM = O_HEAD + K2_NG * 4;
constexpr int K2_SMEM = O_WSUM + 64;
static_assert(K2_SMEM <= 163840, "LDS");
static_assert(K2_ICAP % 8 == 0, "align");
constexpr unsigned SRC_MASK = 0x3fffffu;
constexpr unsigned F_FIN = 0x80000000u, F_SPLIT = 0x40000000u;
#define OUT_AUX 0

DI void masked_zero8(float* d, unsigned long long mask) {
    unsigned long long sv;
    asm volatile("s_and_saveexec_b64 %8, %9\n\ts_cbranch_execz .Lmy%=\n\t"
                 "v_mov_b32 %0, 0\n\tv_mov_b32 %1, 0\n\tv_mov_b32 %2, 0\n\tv_mov_b32 %3, 0\n\t"
                 "v_mov_b32 %4, 0\n\tv_mov_b32 %5, 0\n\tv_mov_b32 %6, 0\n\tv_mov_b32 %7, 0\n"
                 ".Lmy%=:\n\ts_mov_b64 exec, %8"
                 : "+v"(d[0]), "+v"(d[1]), "+v"(d[2]), "+v"(d[3]), "+v"(d[4]), "+v"(d[5]), "+v"(d[6]), "+v"(d[7]), "=&s"(sv)
                 : "s"(mask) : "scc");
}

__global__ __launch_bounds__(K2_THREADS) void painn_k2(const unsigned char* __restrict__ svb,
                                                       const unsigned char* __restrict__ TP,
                                                       const unsigned short* __restrict__ wrbf,
                                                       const Rec* __restrict__ rec,
                                                       const unsigned* __restrict__ tab,
                                                       unsigned char* __restrict__ outb, int N, int BN, int NB, int nchunks) {
    extern __shared__ __attribute__((aligned(16))) char smem[];
    const int tid = threadIdx.x;
    const int lane = tid & 63;
    const int w = __builtin_amdgcn_readfirstlane(tid >> 6);
    const int hf = blockIdx.x & 1;
    const int bin = blockIdx.x >> 1;
    STAMP2(0); STAMPX(2048);
    const int node0 = bin * BN;
    const int nn = min(BN, N - node0);
    unsigned* hist = reinterpret_cast<unsigned*>(smem + O_RING);
    u32x4* items = reinterpret_cast<u32x4*>(smem + O_ITEMS);
    unsigned short* perm = reinterpret_cast<unsigned short*>(smem + O_PERM);
    unsigned* skey = reinterpret_cast<unsigned*>(smem + O_SKEY);
    unsigned* noff = reinterpret_cast<unsigned*>(smem + O_NOFF);
    unsigned* headinfo = reinterpret_cast<unsigned*>(smem + O_HEAD);
    unsigned* wsum = reinterpret_cast<unsigned*>(smem + O_WSUM);

    const __amdgpu_buffer_rsrc_t rTP = __builtin_amdgcn_make_buffer_rsrc((void*)TP, 0, (unsigned)N * 1024u, 0x00020000);
    const __amdgpu_buffer_rsrc_t rSV = __builtin_amdgcn_make_buffer_rsrc((void*)svb, 0, (unsigned)N * 1024u, 0x00020000);
    const __amdgpu_buffer_rsrc_t rOut = __builtin_amdgcn_make_buffer_rsrc((void*)outb, 0, (unsigned)N * 1024u, 0x00020000);
    const __amdgpu_buffer_rsrc_t rRec =
        __builtin_amdgcn_make_buffer_rsrc((void*)rec, 0, (unsigned)nchunks * CH * 32u, 0x00020000);

    const int ncp = (nchunks + 3) & ~3;
    u32x4 e = {0u, 0u, 0u, 0u};
    if (4 * lane < nchunks) e = *reinterpret_cast<const u32x4*>(tab + (size_t)bin * ncp + 4 * lane);
#pragma unroll
    for (int q = 1; q < 4; ++q) e[q] = 4 * lane + q < nchunks ? e[q] : 0u;
    unsigned wpk[3][4];
#pragma unroll
    for (int sg = 0; sg < 3; ++sg)
#pragma unroll
        for (int k = 0; k < 4; ++k)
            wpk[sg][k] = *reinterpret_cast<const unsigned*>(wrbf + k * D3 + sg * D + 64 * hf + 2 * (lane & 31));
    if (tid < 16 * K2_BN / 4) reinterpret_cast<u32x4*>(hist)[tid] = u32x4{0u, 0u, 0u, 0u};
    if (tid < 2) items[2 * K2_CAP + tid] = u32x4{0u, 0u, 0u, 0u};
    if (tid < K2_NG) headinfo[tid] = 0xffffffffu;
    STAMP2(1);
    int T;
    {
        const unsigned c0 = e[0] >> 16, c1 = e[1] >> 16, c2 = e[2] >> 16, c3 = e[3] >> 16;
        const unsigned incl = wave_incl_scan(c0 + c1 + c2 + c3, lane);
        T = __builtin_amdgcn_readlane((int)incl, 63);
        const unsigned o3 = incl - c3, o2 = o3 - c2, o1 = o2 - c1, o0 = o1 - c0;
        const unsigned me = (w & 3) == 0 ? e[0] : (w & 3) == 1 ? e[1] : (w & 3) == 2 ? e[2] : e[3];
        const unsigned mo = (w & 3) == 0 ? o0 : (w & 3) == 1 ? o1 : (w & 3) == 2 ? o2 : o3;
        const unsigned ib = lds_addr(items);
        if (T <= K2_CAP) {
#pragma unroll 1
            for (int l = w >> 2; 4 * l + (w & 3) < nchunks; l += 4) {
                const unsigned ee = (unsigned)__builtin_amdgcn_readlane((int)me, l);
                const unsigned o = (unsigned)__builtin_amdgcn_readlane((int)mo, l);
                const unsigned cnt2 = 2u * (ee >> 16), gb = ((unsigned)(4 * l + (w & 3)) * CH + (ee & 0xffffu)) * 32u;
#pragma unroll 1
                for (unsigned i = 0; i < cnt2; i += 64u)
                    if (i + (unsigned)lane < cnt2) dma16(rRec, gb + (i + (unsigned)lane) * 16u, ib + o * 32u + i * 16u);
            }
        }
    }
    const int Tc = T <= K2_CAP ? T : 0;
    const int M = Tc + nn;
    const int L = (M + K2_NG - 1) / K2_NG;
    asm volatile("s_waitcnt vmcnt(0)" ::: "memory");
    __syncthreads();
    STAMP2(2);
    unsigned* myhist = hist + w * K2_BN;
    unsigned rdk = 0, rkey = 0;
    if (tid < Tc) {
        rkey = items[2 * tid][0];
        const unsigned dl = rkey >> 22;
        rdk = dl | (atomicAdd(&myhist[dl], 1u) << 16);
    }
    __syncthreads();
    STAMP2(4);
    if (w < 2) {
        unsigned ntot = 0;
        if (tid < K2_BN) {
            unsigned h[16];
#pragma unroll
            for (int ww = 0; ww < 16; ++ww) h[ww] = hist[ww * K2_BN + tid];
#pragma unroll
            for (int ww = 0; ww < 16; ++ww) { hist[ww * K2_BN + tid] = ntot; ntot += h[ww]; }
        }
        const unsigned len = tid < nn ? ntot + 1u : 0u;
        const unsigned nincl = wave_incl_scan(len, lane);
        if (lane == 63) wsum[w] = nincl;
        noff[tid] = (nincl - len) | (ntot << 16);
    }
    __syncthreads();
    STAMP2(5);
    const unsigned w0tot = wsum[0];
    const unsigned Mdiv = 0xffffffffu / (unsigned)L + 1u;
    if (tid < Tc) {
        const unsigned dl = rdk & 0xffffu;
        const unsigned p = (noff[dl] & 0xffffu) + (dl >= 64u ? w0tot : 0u) + myhist[dl] + (rdk >> 16);
        perm[p] = (unsigned short)tid;
        skey[p] = rkey & SRC_MASK;
    }
    if (tid < nn) {
        const unsigned no = noff[tid];
        const unsigned p0 = (no & 0xffffu) + (tid >= 64 ? w0tot : 0u), pf = p0 + (no >> 16);
        const unsigned g0 = L == 1 ? p0 : __umulhi(p0, Mdiv), gf = L == 1 ? pf : __umulhi(pf, Mdiv);
        perm[pf] = (unsigned short)K2_CAP;
        skey[pf] = (unsigned)(node0 + tid) | F_FIN | (g0 < gf ? F_SPLIT : 0u);
        if (g0 < gf) headinfo[gf] = g0;
    }
    if (tid >= 512 && M + (tid - 512) < K2_NG * L + 16) {
        perm[M + tid - 512] = (unsigned short)K2_CAP;
        skey[M + tid - 512] = 0u;
    }
    float wr[3][4][2];
#pragma unroll
    for (int sg = 0; sg < 3; ++sg)
#pragma unroll
        for (int k = 0; k < 4; ++k) { wr[sg][k][0] = bf_lo(wpk[sg][k]); wr[sg][k][1] = bf_hi(wpk[sg][k]); }
    __syncthreads();
    STAMP2(9);

    float acc[8], accH[8];
#pragma unroll
    for (int r = 0; r < 8; ++r) { acc[r] = 0.f; accH[r] = 0.f; }
    unsigned hnode = 0xffffffffu;
    const int gC = lane >> 5, l32 = lane & 31;
    const int gD = lane >= 24 ? 1 : 0, lD = lane - 24 * gD;
    const unsigned short* pp = perm + (2 * w + gC) * L;
    const unsigned* pkC = skey + (2 * w + gC) * L;
    const unsigned* pkD = skey + (2 * w + gD) * L;
    const unsigned offP = (unsigned)((lD >> 3) * 256 + hf * 128 + (lD & 7) * 16);
    const unsigned offS = lD < 8 ? (unsigned)(768 + hf * 128 + lD * 16) : 0x80000000u;
    const unsigned offV = offP + 256u;
    const unsigned ringb = lds_addr(smem + O_RING + w * (K2_R * K2_SLOT));
    const char* ringp = smem + O_RING + w * (K2_R * K2_SLOT) + gC * 384 + l32 * 4;

    auto issue = [&](unsigned key, int slot) LAM {
        const unsigned ro = (key & SRC_MASK) * 1024u;
        if (lane < 48) {
            dma16(rTP, ro + ((key & F_FIN) ? offS : offP), ringb + (unsigned)slot * K2_SLOT);
            dma16(rSV, ro + offV, ringb + (unsigned)slot * K2_SLOT + 768u);
        }
    };
    auto compute = [&](int slot, unsigned q, unsigned key) LAM {
        const u32x4 rr = items[2 * q], kk = items[2 * q + 1];
        const unsigned* dp = reinterpret_cast<const unsigned*>(ringp + slot * K2_SLOT);
        asm volatile("s_waitcnt vmcnt(%0)" :: "n"(2 * (K2_R - 1)) : "memory");
        const unsigned ps = dp[0], pv = dp[32], pr = dp[64], vx = dp[192], vy = dp[224], vz = dp[256];
        const float hx = __uint_as_float(rr[1]), hy = __uint_as_float(rr[2]), hz = __uint_as_float(rr[3]);
        const float c0 = __uint_as_float(kk[0]), c1 = __uint_as_float(kk[1]), c2 = __uint_as_float(kk[2]), c3 = __uint_as_float(kk[3]);
        float u[8];
#pragma unroll
        for (int j = 0; j < 2; ++j) {
            float wfs = mul_s(c0, wr[0][0][j]), wfv = mul_s(c0, wr[1][0][j]), wfr = mul_s(c0, wr[2][0][j]);
            fmac_s(wfs, c1, wr[0][1][j]); fmac_s(wfv, c1, wr[1][1][j]); fmac_s(wfr, c1, wr[2][1][j]);
            fmac_s(wfs, c2, wr[0][2][j]); fmac_s(wfv, c2, wr[1][2][j]); fmac_s(wfr, c2, wr[2][2][j]);
            fmac_s(wfs, c3, wr[0][3][j]); fmac_s(wfv, c3, wr[1][3][j]); fmac_s(wfr, c3, wr[2][3][j]);
            u[j] = j ? bf_hi(ps) : bf_lo(ps);
            u[2 + j] = j ? bf_hi(vx) : bf_lo(vx);
            u[4 + j] = j ? bf_hi(vy) : bf_lo(vy);
            u[6 + j] = j ? bf_hi(vz) : bf_lo(vz);
            const float mv = mul_s(j ? bf_hi(pv) : bf_lo(pv), wfv);
            const float mr = mul_s(j ? bf_hi(pr) : bf_lo(pr), wfr);
            fmac_s(acc[j], u[j], wfs);
            fmac_s(acc[2 + j], mv, u[2 + j]); fmac_s(acc[4 + j], mv, u[4 + j]); fmac_s(acc[6 + j], mv, u[6 + j]);
            fmac_s(acc[2 + j], mr, hx); fmac_s(acc[4 + j], mr, hy); fmac_s(acc[6 + j], mr, hz);
        }
        const bool fin = (int)key < 0;
        if (__ballot(fin) != 0ull) {
            float t[8];
#pragma unroll
            for (int r = 0; r < 8; ++r) t[r] = acc[r] + u[r];
            const bool split = (key & F_SPLIT) != 0u;
            const unsigned oo = (fin && !split) ? (key & SRC_MASK) * 1024u + (unsigned)(hf * 128 + l32 * 4) : 0x80000000u;
#pragma unroll
            for (int r = 0; r < 4; ++r)
                __builtin_amdgcn_raw_buffer_store_b32(pack_bf16(t[2 * r], t[2 * r + 1]), rOut, oo + (unsigned)r * 256u, 0, OUT_AUX);
            masked_copy8(accH, t, __ballot(fin && split));
            hnode = (fin && split) ? (key & SRC_MASK) : hnode;
            masked_zero8(acc, __ballot(fin));
        }
    };

    {
#pragma unroll
        for (int k = 0; k < K2_R - 1; ++k) issue(pkD[k], k);
        unsigned kD = pkD[K2_R - 1], q = pp[0], kC = pkC[0];
        int slot = 0, fslot = K2_R - 1;
#pragma unroll 1
        for (int it = 0; it < L; ++it) {
            const unsigned kDn = pkD[it + K2_R], qn = pp[it + 1], kCn = pkC[it + 1];
            issue(kD, fslot);
            compute(slot, q, kC);
            fslot = slot;
            slot = slot + 1 == K2_R ? 0 : slot + 1;
            kD = kDn; q = qn; kC = kCn;
        }
        asm volatile("s_waitcnt vmcnt(0)" ::: "memory");
    }
    STAMP2(10);
    __syncthreads();
    STAMP2(11);
    {
        float* tl = reinterpret_cast<float*>(smem + O_RING);
        const int G = 2 * w + gC;
#pragma unroll
        for (int r = 0; r < 8; ++r) tl[(G * 8 + r) * 32 + l32] = acc[r];
        __syncthreads();
        const unsigned g0 = headinfo[G];
        if (g0 != 0xffffffffu) {
            float t[8];
#pragma unroll
            for (int r = 0; r < 8; ++r) t[r] = tl[(g0 * 8 + r) * 32 + l32];
#pragma unroll 1
            for (int gg = (int)g0 + 1; gg < G; ++gg) {
#pragma unroll
                for (int r = 0; r < 8; ++r) t[r] += tl[(gg * 8 + r) * 32 + l32];
            }
            const unsigned oo = hnode * 1024u + (unsigned)(hf * 128 + l32 * 4);
#pragma unroll
            for (int r = 0; r < 4; ++r)
                __builtin_amdgcn_raw_buffer_store_b32(pack_bf16(t[2 * r] + accH[2 * r], t[2 * r + 1] + accH[2 * r + 1]), rOut,
                                                      oo + (unsigned)r * 256u, 0, OUT_AUX);
        }
    }
    STAMP2(13);
}

extern "C" void kernel_launch(void* const* d_in, const int* in_sizes, int n_in, void* d_out, int out_size,
                              void* d_ws, size_t ws_size, hipStream_t stream) {
    const unsigned short* sv = (const unsigned short*)d_in[0];
    const unsigned char* pos = (const unsigned char*)d_in[1];
    const int* src = (const int*)d_in[2];
    const int* dst = (const int*)d_in[3];
    const unsigned short* freqs = (const unsigned short*)d_in[4];
    const unsigned short* w1 = (const unsigned short*)d_in[5];
    const unsigned short* w2 = (const unsigned short*)d_in[6];
    const unsigned short* wrbf = (const unsigned short*)d_in[7];
    const int N = in_sizes[0] / (4 * D);
    const int E = in_sizes[2];

    int BN = (N + 127) / 128;
    if (BN > K2_BN) BN = K2_BN;
    const int NB = (N + BN - 1) / BN;
    const int nchunks = (E + CH - 1) / CH;
    const int ngemm = (N + G_ROWS - 1) / G_ROWS;
    const unsigned binmagic = (unsigned)(0xffffffffull / (unsigned)BN + 1ull);

    char* ws = (char*)d_ws;
    unsigned char* TP = (unsigned char*)ws;
    size_t off = ((size_t)N * 1024 + 255) & ~(size_t)255;
    Rec* rec = (Rec*)(ws + off);
    off += (size_t)nchunks * CH * sizeof(Rec);
    unsigned* tab = (unsigned*)(ws + off);

    (void)hipFuncSetAttribute((const void*)painn_k1, hipFuncAttributeMaxDynamicSharedMemorySize, K1_SMEM);
    painn_k1<<<ngemm + nchunks, K1_THREADS, K1_SMEM, stream>>>(sv, pos, src, dst, freqs, w1, w2, TP, rec, tab, N, E,
                                                                BN, NB, nchunks, ngemm, binmagic);
    (void)hipFuncSetAttribute((const void*)painn_k2, hipFuncAttributeMaxDynamicSharedMemorySize, K2_SMEM);
    painn_k2<<<NB * 2, K2_THREADS, K2_SMEM, stream>>>((const unsigned char*)sv, TP, wrbf, rec, tab, (unsigned char*)d_out, N, BN, NB,
                                                      nchunks);
}
```

```cpp
#include <hip/hip_runtime.h>
#include <hip/hip_bf16.h>
#include <stdint.h>

using bf16x8 = __attribute__((ext_vector_type(8))) short;
using f32x4 = __attribute__((ext_vector_type(4))) float;
using u32x4 = __attribute__((ext_vector_type(4))) unsigned;
using u32x2 = __attribute__((ext_vector_type(2))) unsigned;
using i32x4 = __attribute__((ext_vector_type(4))) int;
typedef __bf16 bf16x2_t __attribute__((ext_vector_type(2)));
typedef float f32x2 __attribute__((ext_vector_type(2)));

#define DI __device__ __forceinline__
#define LAM __attribute__((always_inline))
DI float fma_s(float a, float b, float c) { float d; asm("v_fma_f32 %0, %1, %2, %3" : "=v"(d) : "v"(a), "v"(b), "v"(c)); return d; }
DI float mul_s(float a, float b) { float d; asm("v_mul_f32 %0, %1, %2" : "=v"(d) : "v"(a), "v"(b)); return d; }
DI void masked_copy8(float* d, const float* s, unsigned long long mask) {
    unsigned long long sv;
    asm volatile("s_and_saveexec_b64 %8, %17\n\ts_cbranch_execz .Lmc%=\n\t"
                 "v_mov_b32 %0, %9\n\tv_mov_b32 %1, %10\n\tv_mov_b32 %2, %11\n\tv_mov_b32 %3, %12\n\t"
                 "v_mov_b32 %4, %13\n\tv_mov_b32 %5, %14\n\tv_mov_b32 %6, %15\n\tv_mov_b32 %7, %16\n"
                 ".Lmc%=:\n\ts_mov_b64 exec, %8"
                 : "+v"(d[0]), "+v"(d[1]), "+v"(d[2]), "+v"(d[3]), "+v"(d[4]), "+v"(d[5]), "+v"(d[6]), "+v"(d[7]), "=&s"(sv)
                 : "v"(s[0]), "v"(s[1]), "v"(s[2]), "v"(s[3]), "v"(s[4]), "v"(s[5]), "v"(s[6]), "v"(s[7]), "s"(mask)
                 : "scc");
}
DI void masked_zero16(float* d, unsigned long long mask) {
    unsigned long long sv;
    asm volatile("s_and_saveexec_b64 %16, %17\n\ts_cbranch_execz .Lmz%=\n\t"
                 "v_mov_b32 %0, 0\n\tv_mov_b32 %1, 0\n\tv_mov_b32 %2, 0\n\tv_mov_b32 %3, 0\n\t"
                 "v_mov_b32 %4, 0\n\tv_mov_b32 %5, 0\n\tv_mov_b32 %6, 0\n\tv_mov_b32 %7, 0\n\t"
                 "v_mov_b32 %8, 0\n\tv_mov_b32 %9, 0\n\tv_mov_b32 %10, 0\n\tv_mov_b32 %11, 0\n\t"
                 "v_mov_b32 %12, 0\n\tv_mov_b32 %13, 0\n\tv_mov_b32 %14, 0\n\tv_mov_b32 %15, 0\n"
                 ".Lmz%=:\n\ts_mov_b64 exec, %16"
                 : "+v"(d[0]), "+v"(d[1]), "+v"(d[2]), "+v"(d[3]), "+v"(d[4]), "+v"(d[5]), "+v"(d[6]), "+v"(d[7]),
                   "+v"(d[8]), "+v"(d[9]), "+v"(d[10]), "+v"(d[11]), "+v"(d[12]), "+v"(d[13]), "+v"(d[14]), "+v"(d[15]), "=&s"(sv)
                 : "s"(mask) : "scc");
}
DI void fmac_s(float& d, float a, float b) { asm("v_fmac_f32 %0, %1, %2" : "+v"(d) : "v"(a), "v"(b)); }
#define STAMP(i)
#define STAMP2(i)
#define STAMPW(i)
#define STAMPW16()
#define STAMPX(i)

constexpr int D = 128;
constexpr int D3 = 384;

DI float bf_lo(unsigned u) { return __uint_as_float(u << 16); }
DI float bf_hi(unsigned u) { return __uint_as_float(u & 0xffff0000u); }
DI unsigned pack_bf16(float lo, float hi) {
    f32x2 v = {lo, hi};
    bf16x2_t b = __builtin_convertvector(v, bf16x2_t);
    return __builtin_bit_cast(unsigned, b);
}
DI float silu_f(float x) { return x * __builtin_amdgcn_rcpf(1.0f + __expf(-x)); }

struct Rec {
    unsigned key;
    float c[4];
    float rh[3];
};

typedef __attribute__((address_space(3))) void* lds_ptr_t;
typedef short s16x4 __attribute__((ext_vector_type(4)));
typedef __attribute__((address_space(3))) s16x4* lds_s16x4_ptr;

DI unsigned wave_incl_scan(unsigned v, int lane) {
    v += (unsigned)__builtin_amdgcn_update_dpp(0, (int)v, 0x111, 0xf, 0xf, false);
    v += (unsigned)__builtin_amdgcn_update_dpp(0, (int)v, 0x112, 0xf, 0xf, false);
    v += (unsigned)__builtin_amdgcn_update_dpp(0, (int)v, 0x114, 0xf, 0xf, false);
    v += (unsigned)__builtin_amdgcn_update_dpp(0, (int)v, 0x118, 0xf, 0xf, false);
    v += (unsigned)__builtin_amdgcn_update_dpp(0, (int)v, 0x142, 0xa, 0xf, false);
    v += (unsigned)__builtin_amdgcn_update_dpp(0, (int)v, 0x143, 0xc, 0xf, false);
    return v;
}
DI unsigned mbcnt64(unsigned long long m) {
    return __builtin_amdgcn_mbcnt_hi((unsigned)(m >> 32), __builtin_amdgcn_mbcnt_lo((unsigned)m, 0));
}
DI void dma16(const __amdgpu_buffer_rsrc_t rsrc, unsigned voff, unsigned lds_dst) {
    unsigned keep;
    asm volatile("s_mov_b32 %0, m0\n\ts_mov_b32 m0, %3\n\ts_nop 0\n\tbuffer_load_dwordx4 %1, %2, 0 offen lds\n\ts_mov_b32 m0, %0"
                 : "=&s"(keep) : "v"(voff), "s"(rsrc), "s"(lds_dst) : "memory");
}
#define GATHER_BITS "sc1"
DI void dma16g(const __amdgpu_buffer_rsrc_t rsrc, unsigned voff, unsigned lds_dst) {
    unsigned keep;
    asm volatile("s_mov_b32 %0, m0\n\ts_mov_b32 m0, %3\n\ts_nop 0\n\tbuffer_load_dwordx4 %1, %2, 0 offen " GATHER_BITS " lds\n\ts_mov_b32 m0, %0"
                 : "=&s"(keep) : "v"(voff), "s"(rsrc), "s"(lds_dst) : "memory");
}
DI unsigned lds_addr(const void* p) { return (unsigned)(size_t)(lds_ptr_t)p; }

constexpr int K1_THREADS = 512;
constexpr int G_TILES = 5;
constexpr int G_ROWS = 16 * G_TILES;
constexpr int HP = 272;
constexpr int GO_S = 131072;
constexpr int GO_H = 0;
constexpr int GO_OST = 32768;
constexpr int OP = 1040;
constexpr int K1_SMEM = 163840;
#define ST_AUX 16

DI unsigned wswz(unsigned row) { return ((row & 3u) << 2) | ((row >> 2) & 3u); }

DI void gemm_block(int blk, const unsigned short* __restrict__ sv, const unsigned short* __restrict__ w1,
                   const unsigned short* __restrict__ w2, unsigned char* __restrict__ TP, int N, char* smem) {
    const int tid = threadIdx.x;
    const int lane = tid & 63;
    const int w = __builtin_amdgcn_readfirstlane(tid >> 6);
    const int i16 = lane & 15, g = lane >> 4;
    const int row0 = blk * G_ROWS;
    const unsigned sm = lds_addr(smem);
    STAMP(0);
    const __amdgpu_buffer_rsrc_t rW1 = __builtin_amdgcn_make_buffer_rsrc((void*)w1, 0, D * D * 2, 0x00020000);
    const __amdgpu_buffer_rsrc_t rW2 = __builtin_amdgcn_make_buffer_rsrc((void*)w2, 0, D * D3 * 2, 0x00020000);
    const __amdgpu_buffer_rsrc_t rSV = __builtin_amdgcn_make_buffer_rsrc((void*)sv, 0, (unsigned)N * 1024u, 0x00020000);
    const unsigned rot = (unsigned)blk * 5u;

#pragma unroll
    for (int q = 0; q < 4; ++q) {
        const unsigned i = ((unsigned)(w + 8 * q) + rot) & 31u;
        const unsigned row = 4u * i + (unsigned)g;
        dma16(rW1, row * 256u + (((unsigned)i16 ^ wswz(row)) * 16u), sm + i * 1024u);
    }
#pragma unroll
    for (int q = 0; q < 3; ++q) {
        const unsigned i = (unsigned)(w + 8 * q);
        if (i < G_ROWS / 4) {
            const unsigned node = 4u * i + (unsigned)g;
            dma16(rSV, ((unsigned)row0 + node) * 1024u + (((unsigned)i16 ^ (node & 15u)) * 16u), sm + GO_S + i * 1024u);
        }
    }
#pragma unroll
    for (int q = 0; q < 12; ++q) {
        const unsigned i = ((unsigned)(w + 8 * (q & 3)) + rot) & 31u, seg = (unsigned)(q >> 2);
        const unsigned row = 4u * i + (unsigned)g;
        dma16(rW2, row * 768u + seg * 256u + (((unsigned)i16 ^ wswz(row)) * 16u), sm + 32768u * (seg + 1u) + i * 1024u);
    }
    asm volatile("s_waitcnt vmcnt(12)" ::: "memory");
    __syncthreads();
    STAMP(1);
    const unsigned tq = (unsigned)i16 >> 2, tp = (unsigned)lane & 3u;
    auto afrag = [&](int im, int ks) LAM {
        const unsigned row = 32u * ks + 8u * g + tq;
        const s16x4 lo = __builtin_amdgcn_ds_read_tr16_b64_v4i16((lds_s16x4_ptr)(smem + im * 32768 + 256u * row +
                            16u * ((2u * w + (tp >> 1)) ^ wswz(row)) + 8u * (tp & 1u)));
        const s16x4 hi = __builtin_amdgcn_ds_read_tr16_b64_v4i16((lds_s16x4_ptr)(smem + im * 32768 + 256u * (row + 4u) +
                            16u * ((2u * w + (tp >> 1)) ^ wswz(row + 4u)) + 8u * (tp & 1u)));
        return bf16x8{lo[0], lo[1], lo[2], lo[3], hi[0], hi[1], hi[2], hi[3]};
    };
    bf16x8 a1[4];
#pragma unroll
    for (int ks = 0; ks < 4; ++ks) a1[ks] = afrag(0, ks);
    __syncthreads();
#pragma unroll 1
    for (int t = 0; t < G_TILES; ++t) {
        const char* st = smem + GO_S + (16 * t + i16) * 256;
        f32x4 c = {0.f, 0.f, 0.f, 0.f};
#pragma unroll
        for (int ks = 0; ks < 4; ++ks) {
            const bf16x8 b = *reinterpret_cast<const bf16x8*>(st + (((unsigned)(4 * ks + g) ^ (unsigned)i16) * 16u));
            c = __builtin_amdgcn_mfma_f32_16x16x32_bf16(a1[ks], b, c, 0, 0, 0);
        }
        u32x2 o;
        o[0] = pack_bf16(silu_f(c[0]), silu_f(c[1]));
        o[1] = pack_bf16(silu_f(c[2]), silu_f(c[3]));
        *reinterpret_cast<u32x2*>(smem + GO_H + (16 * t + i16) * HP + (16 * w + 4 * g) * 2) = o;
    }
    STAMP(2);
    asm volatile("s_waitcnt vmcnt(0)" ::: "memory");
    __syncthreads();
    bf16x8 a2[3][4];
#pragma unroll
    for (int q = 0; q < 3; ++q)
#pragma unroll
        for (int ks = 0; ks < 4; ++ks) a2[q][ks] = afrag(q + 1, ks);
    __syncthreads();
    STAMP(3);
    const __amdgpu_buffer_rsrc_t rT = __builtin_amdgcn_make_buffer_rsrc((void*)TP, 0, (unsigned)N * 1024u, 0x00020000);
    const unsigned sw = ((((unsigned)(2 * w + (g >> 1))) ^ (unsigned)i16) * 16u) + (unsigned)(g & 1) * 8u;
    char* ost = smem + GO_OST;
#pragma unroll 1
    for (int t = 0; t < G_TILES; ++t) {
        char* orow = ost + (16 * t + i16) * OP + 32 * w + 8 * g;
        bf16x8 bh[4];
#pragma unroll
        for (int ks = 0; ks < 4; ++ks)
            bh[ks] = *reinterpret_cast<const bf16x8*>(smem + GO_H + (16 * t + i16) * HP + (32 * ks + 8 * g) * 2);
        *reinterpret_cast<u32x2*>(orow + 768) = *reinterpret_cast<const u32x2*>(smem + GO_S + (16 * t + i16) * 256 + sw);
        f32x4 c[3];
#pragma unroll
        for (int q = 0; q < 3; ++q) c[q] = f32x4{0.f, 0.f, 0.f, 0.f};
#pragma unroll
        for (int ks = 0; ks < 4; ++ks)
#pragma unroll
            for (int q = 0; q < 3; ++q) c[q] = __builtin_amdgcn_mfma_f32_16x16x32_bf16(a2[q][ks], bh[ks], c[q], 0, 0, 0);
#pragma unroll
        for (int q = 0; q < 3; ++q) {
            u32x2 o;
            o[0] = pack_bf16(c[q][0], c[q][1]);
            o[1] = pack_bf16(c[q][2], c[q][3]);
            *reinterpret_cast<u32x2*>(orow + q * 256) = o;
        }
    }
    STAMP(4);
    __syncthreads();
#pragma unroll 2
    for (int r = w; r < G_ROWS; r += 8) {
        const u32x4 o = *reinterpret_cast<const u32x4*>(ost + r * OP + lane * 16);
        __builtin_amdgcn_raw_buffer_store_b128(o, rT, (unsigned)(row0 + r) * 1024u + (unsigned)lane * 16u, 0, ST_AUX);
    }
    STAMP(5);
}

constexpr int EPT = 8;
constexpr int CH = K1_THREADS * EPT;
constexpr int MAXNB = 128;
constexpr int EO_POS = 0;
constexpr int POS_LDS_MAX = 98304;
constexpr int EO_SRC = POS_LDS_MAX;
constexpr int EO_DST = EO_SRC + CH * 4;
constexpr int EO_RANK = EO_DST + CH * 4;
constexpr int EO_HIST = EO_RANK + CH * 2;
static_assert(EO_HIST + 8 * MAXNB * 4 <= K1_SMEM, "LDS");

struct Pos3 { float x, y, z; };
DI Pos3 unpack_pos(unsigned a, unsigned b, unsigned odd16) {
    const unsigned lo = __builtin_amdgcn_alignbit(b, a, odd16);
    const unsigned hi = b >> odd16;
    return Pos3{bf_lo(lo), bf_hi(lo), bf_lo(hi)};
}
DI Pos3 pos_lds(const char* lp, unsigned n) {
    const unsigned off = 6u * n;
    const unsigned* p = reinterpret_cast<const unsigned*>(lp + (off & ~3u));
    return unpack_pos(p[0], p[1], (off & 2u) << 3);
}
DI Pos3 pos_glb(const unsigned char* __restrict__ posb, unsigned n, unsigned lim) {
    const unsigned off = 6u * n;
    const unsigned a = off < lim ? off : lim;
    unsigned long long v;
    __builtin_memcpy(&v, posb + a, 8);
    v >>= (off - a) * 8u;
    return Pos3{bf_lo((unsigned)v), bf_hi((unsigned)v), bf_lo((unsigned)(v >> 32))};
}
DI float sin_rev(float r) { return __builtin_amdgcn_sinf(r - __builtin_rintf(r)); }
DI float cos_rev(float r) { return __builtin_amdgcn_cosf(r - __builtin_rintf(r)); }

template <bool POS_LDS>
DI void edge_block(int chunk, int nchunks, const int* __restrict__ src, const int* __restrict__ dst,
                   const unsigned char* __restrict__ posb, const unsigned short* __restrict__ freqs,
                   Rec* __restrict__ rec, unsigned* __restrict__ tab, int N, int E,
                   int BN, int NB, unsigned binmagic, char* smem) {
    const int tid = threadIdx.x;
    const int lane = tid & 63;
    const int w = __builtin_amdgcn_readfirstlane(tid >> 6);
    unsigned* it_src = reinterpret_cast<unsigned*>(smem + EO_SRC);
    unsigned* it_dst = reinterpret_cast<unsigned*>(smem + EO_DST);
    unsigned short* it_rank = reinterpret_cast<unsigned short*>(smem + EO_RANK);
    unsigned* hist = reinterpret_cast<unsigned*>(smem + EO_HIST);
    const unsigned lim = 6u * (unsigned)N - 8u;
    STAMP(0);
    const int wbase = chunk * CH + w * (EPT * 64);
    int es[EPT], ed[EPT];
#pragma unroll
    for (int j = 0; j < EPT / 4; ++j) {
        const int e = wbase + (j * 64 + lane) * 4;
        if (e + 3 < E) {
            const i32x4 s4 = *reinterpret_cast<const i32x4*>(src + e);
            const i32x4 d4 = *reinterpret_cast<const i32x4*>(dst + e);
#pragma unroll
            for (int q = 0; q < 4; ++q) { es[4 * j + q] = s4[q]; ed[4 * j + q] = d4[q]; }
        } else {
#pragma unroll
            for (int q = 0; q < 4; ++q) {
                es[4 * j + q] = (e + q < E) ? src[e + q] : -1;
                ed[4 * j + q] = (e + q < E) ? dst[e + q] : 0;
            }
        }
    }
    if (POS_LDS) {
        const unsigned bytes = 6u * (unsigned)N;
        const __amdgpu_buffer_rsrc_t rP = __builtin_amdgcn_make_buffer_rsrc((void*)posb, 0, bytes & ~3u, 0x00020000);
        const unsigned sm = lds_addr(smem);
        for (unsigned o = (unsigned)w * 1024u; o < bytes; o += 8192u) dma16(rP, o + (unsigned)lane * 16u, sm + EO_POS + o);
    }
    hist[tid] = 0; hist[tid + K1_THREADS] = 0;
    if (POS_LDS) {
        asm volatile("s_waitcnt vmcnt(0)" ::: "memory");
        __syncthreads();
        if ((N & 1) && tid == 0)
            *reinterpret_cast<unsigned*>(smem + EO_POS + ((6u * (unsigned)N) & ~3u)) =
                (unsigned)*reinterpret_cast<const unsigned short*>(posb + 6u * (unsigned)N - 2u);
        if (N & 1) __syncthreads();
    } else {
        __syncthreads();
    }
    STAMP(1);
    int cnt = 0;
#pragma unroll
    for (int j = 0; j < EPT; ++j) {
        const unsigned s = (unsigned)(es[j] < 0 ? 0 : es[j]), d = (unsigned)ed[j];
        const Pos3 a = POS_LDS ? pos_lds(smem + EO_POS, s) : pos_glb(posb, s, lim);
        const Pos3 b = POS_LDS ? pos_lds(smem + EO_POS, d) : pos_glb(posb, d, lim);
        const float rx = a.x - b.x, ry = a.y - b.y, rz = a.z - b.z;
        const float d2 = rx * rx + ry * ry + rz * rz + 1e-8f;
        const bool act = (es[j] >= 0) && !(d2 >= 25.0f && d2 < __builtin_inff());
        const unsigned long long m = __ballot(act);
        if (act) {
            const int p = w * (EPT * 64) + cnt + (int)mbcnt64(m);
            it_src[p] = s;
            it_dst[p] = d;
        }
        cnt += __popcll(m);
    }
    STAMP(2);
    unsigned* myhist = hist + w * MAXNB;
#pragma unroll 1
    for (int b = lane; b < cnt; b += 64) {
        const int p = w * (EPT * 64) + b;
        it_rank[p] = (unsigned short)atomicAdd(&myhist[__umulhi(it_dst[p], binmagic)], 1u);
    }
    __syncthreads();
    if (w == 0) {
        u32x2 h[8];
        unsigned t0 = 0, t1 = 0;
#pragma unroll
        for (int ww = 0; ww < 8; ++ww) {
            h[ww] = *reinterpret_cast<const u32x2*>(hist + ww * MAXNB + 2 * lane);
            t0 += h[ww][0]; t1 += h[ww][1];
        }
        unsigned r0 = wave_incl_scan(t0 + t1, lane) - t0 - t1, r1 = r0 + t0;
        const int ncp = (nchunks + 3) & ~3;
        if (2 * lane < NB) tab[(size_t)(2 * lane) * ncp + chunk] = r0 | (t0 << 16);
        if (2 * lane + 1 < NB) tab[(size_t)(2 * lane + 1) * ncp + chunk] = r1 | (t1 << 16);
#pragma unroll
        for (int ww = 0; ww < 8; ++ww) {
            *reinterpret_cast<u32x2*>(hist + ww * MAXNB + 2 * lane) = u32x2{r0, r1};
            r0 += h[ww][0]; r1 += h[ww][1];
        }
    }
    __syncthreads();
    STAMP(3);
    float fr[4];
#pragma unroll
    for (int k = 0; k < 4; ++k) fr[k] = bf_lo((unsigned)freqs[k]) * 0.15915494309189535f;
#pragma unroll 1
    for (int b = lane; b < cnt; b += 64) {
        const int p = w * (EPT * 64) + b;
        const unsigned s = it_src[p], dd = it_dst[p];
        const Pos3 a = POS_LDS ? pos_lds(smem + EO_POS, s) : pos_glb(posb, s, lim);
        const Pos3 c = POS_LDS ? pos_lds(smem + EO_POS, dd) : pos_glb(posb, dd, lim);
        const float rx = a.x - c.x, ry = a.y - c.y, rz = a.z - c.z;
        const unsigned bin = __umulhi(dd, binmagic);
        const unsigned dl = dd - bin * (unsigned)BN;
        const unsigned pos = myhist[bin] + (unsigned)it_rank[p];
        const float d2 = rx * rx + ry * ry + rz * rz + 1e-8f;
        const float inv = __builtin_amdgcn_rsqf(d2);
        const float d = d2 * inv;
        float fc = 0.5f * (cos_rev(d * 0.1f) + 1.0f);
        fc = (d < 5.0f) ? fc : 0.0f * d;
        const float sc = inv * fc;
        u32x4 r0, r1;
        r0[0] = s | (dl << 22);
        r0[1] = __float_as_uint(rx * inv);
        r0[2] = __float_as_uint(ry * inv);
        r0[3] = __float_as_uint(rz * inv);
        r1[0] = __float_as_uint(sin_rev(d * fr[0]) * sc);
        r1[1] = __float_as_uint(sin_rev(d * fr[1]) * sc);
        r1[2] = __float_as_uint(sin_rev(d * fr[2]) * sc);
        r1[3] = __float_as_uint(sin_rev(d * fr[3]) * sc);
        u32x4* o = reinterpret_cast<u32x4*>(rec + (size_t)chunk * CH + pos);
        o[0] = r0;
        o[1] = r1;
    }
    STAMP(5);
}

__global__ __launch_bounds__(K1_THREADS) void painn_k1(const unsigned short* __restrict__ sv,
                                                       const unsigned char* __restrict__ posb,
                                                       const int* __restrict__ src, const int* __restrict__ dst,
                                                       const unsigned short* __restrict__ freqs,
                                                       const unsigned short* __restrict__ w1,
                                                       const unsigned short* __restrict__ w2,
                                                       unsigned char* __restrict__ TP, Rec* __restrict__ rec,
                                                       unsigned* __restrict__ tab, int N, int E, int BN, int NB,
                                                       int nchunks, int ngemm, unsigned binmagic) {
    extern __shared__ __attribute__((aligned(16))) char smem[];
    const int b = blockIdx.x;
    STAMPX(0);
    if (b < nchunks) {
        if (6 * N + 16 <= POS_LDS_MAX)
            edge_block<true>(b, nchunks, src, dst, posb, freqs, rec, tab, N, E, BN, NB, binmagic, smem);
        else
            edge_block<false>(b, nchunks, src, dst, posb, freqs, rec, tab, N, E, BN, NB, binmagic, smem);
    } else {
        gemm_block(b - nchunks, sv, w1, w2, TP, N, smem);
    }
}

constexpr int K2_NW = 16;
constexpr int K2_THREADS = K2_NW * 64;
constexpr int K2_BN = 100;
constexpr int K2_NG = K2_NW * 2;
constexpr int K2_CAP = 768;
constexpr int K2_ICAP = K2_CAP + K2_BN + K2_NG + 20;
constexpr int K2_MAXCH = 256;
#define RING_R 3
constexpr int K2_R = RING_R;
constexpr int K2_SLOT = 1536;
constexpr int O_RING = 0;
constexpr int O_ITEMS = K2_NW * K2_R * K2_SLOT;
constexpr int O_PERM = O_ITEMS + (K2_CAP + 1) * 32;
constexpr int O_SKEY = O_PERM + K2_ICAP * 2;
constexpr int O_NOFF = O_SKEY + K2_ICAP * 4;
constexpr int O_HEAD = O_NOFF + 128 * 4;
constexpr int O_WSUM = O_HEAD + K2_NG * 4;
constexpr int O_DUMP = O_WSUM + 64;
constexpr int K2_SMEM = O_DUMP + 1024;
static_assert(K2_SMEM <= 163840, "LDS");
static_assert(K2_ICAP % 8 == 0, "align");
constexpr unsigned SRC_MASK = 0x3fffffu;
constexpr unsigned F_FIN = 0x80000000u, F_SPLIT = 0x40000000u;
#define OUT_AUX 2

DI void masked_zero8(float* d, unsigned long long mask) {
    unsigned long long sv;
    asm volatile("s_and_saveexec_b64 %8, %9\n\ts_cbranch_execz .Lmy%=\n\t"
                 "v_mov_b32 %0, 0\n\tv_mov_b32 %1, 0\n\tv_mov_b32 %2, 0\n\tv_mov_b32 %3, 0\n\t"
                 "v_mov_b32 %4, 0\n\tv_mov_b32 %5, 0\n\tv_mov_b32 %6, 0\n\tv_mov_b32 %7, 0\n"
                 ".Lmy%=:\n\ts_mov_b64 exec, %8"
                 : "+v"(d[0]), "+v"(d[1]), "+v"(d[2]), "+v"(d[3]), "+v"(d[4]), "+v"(d[5]), "+v"(d[6]), "+v"(d[7]), "=&s"(sv)
                 : "s"(mask) : "scc");
}

__global__ __launch_bounds__(K2_THREADS) void painn_k2(const unsigned char* __restrict__ svb,
                                                       const unsigned char* __restrict__ TP,
                                                       const unsigned short* __restrict__ wrbf,
                                                       const Rec* __restrict__ rec,
                                                       const unsigned* __restrict__ tab,
                                                       unsigned char* __restrict__ outb, int N, int BN, int NB, int nchunks) {
    extern __shared__ __attribute__((aligned(16))) char smem[];
    const int tid = threadIdx.x;
    const int lane = tid & 63;
    const int w = __builtin_amdgcn_readfirstlane(tid >> 6);
    const int hf = blockIdx.x & 1;
    const int bin = blockIdx.x >> 1;
    STAMP2(0); STAMPX(2048);
    const int node0 = bin * BN;
    const int nn = min(BN, N - node0);
    unsigned* hist = reinterpret_cast<unsigned*>(smem + O_RING);
    u32x4* items = reinterpret_cast<u32x4*>(smem + O_ITEMS);
    unsigned short* perm = reinterpret_cast<unsigned short*>(smem + O_PERM);
    unsigned* skey = reinterpret_cast<unsigned*>(smem + O_SKEY);
    unsigned* noff = reinterpret_cast<unsigned*>(smem + O_NOFF);
    unsigned* headinfo = reinterpret_cast<unsigned*>(smem + O_HEAD);
    unsigned* wsum = reinterpret_cast<unsigned*>(smem + O_WSUM);

    const __amdgpu_buffer_rsrc_t rTP = __builtin_amdgcn_make_buffer_rsrc((void*)TP, 0, (unsigned)N * 1024u, 0x00020000);
    const __amdgpu_buffer_rsrc_t rSV = __builtin_amdgcn_make_buffer_rsrc((void*)svb, 0, (unsigned)N * 1024u, 0x00020000);
    const __amdgpu_buffer_rsrc_t rOut = __builtin_amdgcn_make_buffer_rsrc((void*)outb, 0, (unsigned)N * 1024u, 0x00020000);
    const __amdgpu_buffer_rsrc_t rRec =
        __builtin_amdgcn_make_buffer_rsrc((void*)rec, 0, (unsigned)nchunks * CH * 32u, 0x00020000);

    const int ncp = (nchunks + 3) & ~3;
    u32x4 e = {0u, 0u, 0u, 0u};
    if (4 * lane < nchunks) e = *reinterpret_cast<const u32x4*>(tab + (size_t)bin * ncp + 4 * lane);
#pragma unroll
    for (int q = 1; q < 4; ++q) e[q] = 4 * lane + q < nchunks ? e[q] : 0u;
    unsigned wpk[3][4];
#pragma unroll
    for (int sg = 0; sg < 3; ++sg)
#pragma unroll
        for (int k = 0; k < 4; ++k)
            wpk[sg][k] = *reinterpret_cast<const unsigned*>(wrbf + k * D3 + sg * D + 64 * hf + 2 * (lane & 31));
    if (tid < 16 * K2_BN / 4) reinterpret_cast<u32x4*>(hist)[tid] = u32x4{0u, 0u, 0u, 0u};
    if (tid < 2) items[2 * K2_CAP + tid] = u32x4{0u, 0u, 0u, 0u};
    if (tid < K2_NG) headinfo[tid] = 0xffffffffu;
    STAMP2(1);
    int T;
    {
        const unsigned c0 = e[0] >> 16, c1 = e[1] >> 16, c2 = e[2] >> 16, c3 = e[3] >> 16;
        const unsigned incl = wave_incl_scan(c0 + c1 + c2 + c3, lane);
        T = __builtin_amdgcn_readlane((int)incl, 63);
        const unsigned o3 = incl - c3, o2 = o3 - c2, o1 = o2 - c1, o0 = o1 - c0;
        const unsigned me = (w & 3) == 0 ? e[0] : (w & 3) == 1 ? e[1] : (w & 3) == 2 ? e[2] : e[3];
        const unsigned mo = (w & 3) == 0 ? o0 : (w & 3) == 1 ? o1 : (w & 3) == 2 ? o2 : o3;
        const unsigned ib = lds_addr(items);
        if (T <= K2_CAP) {
#pragma unroll 1
            for (int l = w >> 2; 4 * l + (w & 3) < nchunks; l += 4) {
                const unsigned ee = (unsigned)__builtin_amdgcn_readlane((int)me, l);
                const unsigned o = (unsigned)__builtin_amdgcn_readlane((int)mo, l);
                const unsigned cnt2 = 2u * (ee >> 16), gb = ((unsigned)(4 * l + (w & 3)) * CH + (ee & 0xffffu)) * 32u;
#pragma unroll 1
                for (unsigned i = 0; i < cnt2; i += 64u)
                    if (i + (unsigned)lane < cnt2) dma16(rRec, gb + (i + (unsigned)lane) * 16u, ib + o * 32u + i * 16u);
            }
        }
    }
    const int Tc = T <= K2_CAP ? T : 0;
    const int M = Tc + nn;
    const int L = (M + K2_NG - 1) / K2_NG;
    asm volatile("s_waitcnt vmcnt(0)" ::: "memory");
    __syncthreads();
    STAMP2(2);
    unsigned* myhist = hist + w * K2_BN;
    unsigned rdk = 0, rkey = 0;
    if (tid < Tc) {
        rkey = items[2 * tid][0];
        const unsigned dl = rkey >> 22;
        rdk = dl | (atomicAdd(&myhist[dl], 1u) << 16);
    }
    __syncthreads();
    STAMP2(4);
    if (w < 2) {
        unsigned ntot = 0;
        if (tid < K2_BN) {
            unsigned h[16];
#pragma unroll
            for (int ww = 0; ww < 16; ++ww) h[ww] = hist[ww * K2_BN + tid];
#pragma unroll
            for (int ww = 0; ww < 16; ++ww) { hist[ww * K2_BN + tid] = ntot; ntot += h[ww]; }
        }
        const unsigned len = tid < nn ? ntot + 1u : 0u;
        const unsigned nincl = wave_incl_scan(len, lane);
        if (lane == 63) wsum[w] = nincl;
        noff[tid] = (nincl - len) | (ntot << 16);
    }
    __syncthreads();
    STAMP2(5);
    const unsigned w0tot = wsum[0];
    const unsigned Mdiv = 0xffffffffu / (unsigned)L + 1u;
    if (tid < Tc) {
        const unsigned dl = rdk & 0xffffu;
        const unsigned p = (noff[dl] & 0xffffu) + (dl >= 64u ? w0tot : 0u) + myhist[dl] + (rdk >> 16);
        perm[p] = (unsigned short)tid;
        skey[p] = rkey & SRC_MASK;
    }
    if (tid < nn) {
        const unsigned no = noff[tid];
        const unsigned p0 = (no & 0xffffu) + (tid >= 64 ? w0tot : 0u), pf = p0 + (no >> 16);
        const unsigned g0 = L == 1 ? p0 : __umulhi(p0, Mdiv), gf = L == 1 ? pf : __umulhi(pf, Mdiv);
        perm[pf] = (unsigned short)K2_CAP;
        skey[pf] = (unsigned)(node0 + tid) | F_FIN | (g0 < gf ? F_SPLIT : 0u);
        if (g0 < gf) headinfo[gf] = g0;
    }
    if (tid >= 512 && M + (tid - 512) < K2_NG * L + 16) {
        perm[M + tid - 512] = (unsigned short)K2_CAP;
        skey[M + tid - 512] = 0u;
    }
    float wr[3][4][2];
#pragma unroll
    for (int sg = 0; sg < 3; ++sg)
#pragma unroll
        for (int k = 0; k < 4; ++k) { wr[sg][k][0] = bf_lo(wpk[sg][k]); wr[sg][k][1] = bf_hi(wpk[sg][k]); }
    __syncthreads();
    STAMP2(9);

    float acc[8], accH[8];
#pragma unroll
    for (int r = 0; r < 8; ++r) { acc[r] = 0.f; accH[r] = 0.f; }
    unsigned hnode = 0xffffffffu;
    const int gC = lane >> 5, l32 = lane & 31;
    const int gD = lane >= 24 ? 1 : 0, lD = lane - 24 * gD;
    const unsigned short* pp = perm + (2 * w + gC) * L;
    const unsigned* pkC = skey + (2 * w + gC) * L;
    const unsigned* pkD = skey + (2 * w + gD) * L;
    const unsigned offP = (unsigned)((lD >> 3) * 256 + hf * 128 + (lD & 7) * 16);
    const unsigned offS = lD < 8 ? (unsigned)(768 + hf * 128 + lD * 16) : 0x80000000u;
    const unsigned offV = offP + 256u;
    const unsigned ringb = lds_addr(smem + O_RING + w * (K2_R * K2_SLOT));
    const char* ringp = smem + O_RING + w * (K2_R * K2_SLOT) + gC * 384 + l32 * 4;

    auto issue = [&](unsigned key, int slot) LAM {
        const unsigned ro = (key & SRC_MASK) * 1024u;
        if (lane < 48) {
            dma16g(rTP, ro + ((key & F_FIN) ? offS : offP), ringb + (unsigned)slot * K2_SLOT);
            dma16g(rSV, ro + offV, ringb + (unsigned)slot * K2_SLOT + 768u);
        }
    };
    auto compute = [&](int slot, unsigned q, unsigned key) LAM {
        const u32x4 rr = items[2 * q], kk = items[2 * q + 1];
        const unsigned* dp = reinterpret_cast<const unsigned*>(ringp + slot * K2_SLOT);
        asm volatile("s_waitcnt vmcnt(%0)" :: "n"(2 * (K2_R - 1)) : "memory");
        const unsigned ps = dp[0], pv = dp[32], pr = dp[64], vx = dp[192], vy = dp[224], vz = dp[256];
        const float hx = __uint_as_float(rr[1]), hy = __uint_as_float(rr[2]), hz = __uint_as_float(rr[3]);
        const float c0 = __uint_as_float(kk[0]), c1 = __uint_as_float(kk[1]), c2 = __uint_as_float(kk[2]), c3 = __uint_as_float(kk[3]);
        float u[8];
#pragma unroll
        for (int j = 0; j < 2; ++j) {
            float wfs = mul_s(c0, wr[0][0][j]), wfv = mul_s(c0, wr[1][0][j]), wfr = mul_s(c0, wr[2][0][j]);
            fmac_s(wfs, c1, wr[0][1][j]); fmac_s(wfv, c1, wr[1][1][j]); fmac_s(wfr, c1, wr[2][1][j]);
            fmac_s(wfs, c2, wr[0][2][j]); fmac_s(wfv, c2, wr[1][2][j]); fmac_s(wfr, c2, wr[2][2][j]);
            fmac_s(wfs, c3, wr[0][3][j]); fmac_s(wfv, c3, wr[1][3][j]); fmac_s(wfr, c3, wr[2][3][j]);
            u[j] = j ? bf_hi(ps) : bf_lo(ps);
            u[2 + j] = j ? bf_hi(vx) : bf_lo(vx);
            u[4 + j] = j ? bf_hi(vy) : bf_lo(vy);
            u[6 + j] = j ? bf_hi(vz) : bf_lo(vz);
            const float mv = mul_s(j ? bf_hi(pv) : bf_lo(pv), wfv);
            const float mr = mul_s(j ? bf_hi(pr) : bf_lo(pr), wfr);
            fmac_s(acc[j], u[j], wfs);
            fmac_s(acc[2 + j], mv, u[2 + j]); fmac_s(acc[4 + j], mv, u[4 + j]); fmac_s(acc[6 + j], mv, u[6 + j]);
            fmac_s(acc[2 + j], mr, hx); fmac_s(acc[4 + j], mr, hy); fmac_s(acc[6 + j], mr, hz);
        }
        const bool fin = (int)key < 0;
        if (__ballot(fin) != 0ull) {
            float t[8];
#pragma unroll
            for (int r = 0; r < 8; ++r) t[r] = acc[r] + u[r];
            const bool split = (key & F_SPLIT) != 0u;
            const unsigned oo = (fin && !split) ? (key & SRC_MASK) * 1024u + (unsigned)(hf * 128 + l32 * 4) : 0x80000000u;
#pragma unroll
            for (int r = 0; r < 4; ++r)
                __builtin_amdgcn_raw_buffer_store_b32(pack_bf16(t[2 * r], t[2 * r + 1]), rOut, oo + (unsigned)r * 256u, 0, OUT_AUX);
            masked_copy8(accH, t, __ballot(fin && split));
            hnode = (fin && split) ? (key & SRC_MASK) : hnode;
            masked_zero8(acc, __ballot(fin));
        }
    };

    {
#pragma unroll
        for (int k = 0; k < K2_R - 1; ++k) issue(pkD[k], k);
        unsigned kD = pkD[K2_R - 1], q = pp[0], kC = pkC[0];
        int slot = 0, fslot = K2_R - 1;
#pragma unroll 1
        for (int it = 0; it < L; ++it) {
            const unsigned kDn = pkD[it + K2_R], qn = pp[it + 1], kCn = pkC[it + 1];
            issue(kD, fslot);
            compute(slot, q, kC);
            fslot = slot;
            slot = slot + 1 == K2_R ? 0 : slot + 1;
            kD = kDn; q = qn; kC = kCn;
        }
        asm volatile("s_waitcnt vmcnt(0)" ::: "memory");
    }
    STAMP2(10);
    __syncthreads();
    STAMP2(11);
    {
        float* tl = reinterpret_cast<float*>(smem + O_RING);
        const int G = 2 * w + gC;
#pragma unroll
        for (int r = 0; r < 8; ++r) tl[(G * 8 + r) * 32 + l32] = acc[r];
        __syncthreads();
        const unsigned g0 = headinfo[G];
        if (g0 != 0xffffffffu) {
            float t[8];
#pragma unroll
            for (int r = 0; r < 8; ++r) t[r] = tl[(g0 * 8 + r) * 32 + l32];
#pragma unroll 1
            for (int gg = (int)g0 + 1; gg < G; ++gg) {
#pragma unroll
                for (int r = 0; r < 8; ++r) t[r] += tl[(gg * 8 + r) * 32 + l32];
            }
            const unsigned oo = hnode * 1024u + (unsigned)(hf * 128 + l32 * 4);
#pragma unroll
            for (int r = 0; r < 4; ++r)
                __builtin_amdgcn_raw_buffer_store_b32(pack_bf16(t[2 * r] + accH[2 * r], t[2 * r + 1] + accH[2 * r + 1]), rOut,
                                                      oo + (unsigned)r * 256u, 0, OUT_AUX);
        }
    }
    STAMP2(13);
}

extern "C" void kernel_launch(void* const* d_in, const int* in_sizes, int n_in, void* d_out, int out_size,
                              void* d_ws, size_t ws_size, hipStream_t stream) {
    const unsigned short* sv = (const unsigned short*)d_in[0];
    const unsigned char* pos = (const unsigned char*)d_in[1];
    const int* src = (const int*)d_in[2];
    const int* dst = (const int*)d_in[3];
    const unsigned short* freqs = (const unsigned short*)d_in[4];
    const unsigned short* w1 = (const unsigned short*)d_in[5];
    const unsigned short* w2 = (const unsigned short*)d_in[6];
    const unsigned short* wrbf = (const unsigned short*)d_in[7];
    const int N = in_sizes[0] / (4 * D);
    const int E = in_sizes[2];

    int BN = (N + 127) / 128;
    if (BN > K2_BN) BN = K2_BN;
    const int NB = (N + BN - 1) / BN;
    const int nchunks = (E + CH - 1) / CH;
    const int ngemm = (N + G_ROWS - 1) / G_ROWS;
    const unsigned binmagic = (unsigned)(0xffffffffull / (unsigned)BN + 1ull);

    char* ws = (char*)d_ws;
    unsigned char* TP = (unsigned char*)ws;
    size_t off = ((size_t)N * 1024 + 255) & ~(size_t)255;
    Rec* rec = (Rec*)(ws + off);
    off += (size_t)nchunks * CH * sizeof(Rec);
    unsigned* tab = (unsigned*)(ws + off);

    (void)hipFuncSetAttribute((const void*)painn_k1, hipFuncAttributeMaxDynamicSharedMemorySize, K1_SMEM);
    painn_k1<<<ngemm + nchunks, K1_THREADS, K1_SMEM, stream>>>(sv, pos, src, dst, freqs, w1, w2, TP, rec, tab, N, E,
                                                                BN, NB, nchunks, ngemm, binmagic);
    (void)hipFuncSetAttribute((const void*)painn_k2, hipFuncAttributeMaxDynamicSharedMemorySize, K2_SMEM);
    painn_k2<<<NB * 2, K2_THREADS, K2_SMEM, stream>>>((const unsigned char*)sv, TP, wrbf, rec, tab, (unsigned char*)d_out, N, BN, NB,
                                                      nchunks);
}
```

```cpp
#include <hip/hip_runtime.h>
#include <hip/hip_bf16.h>
#include <stdint.h>

using bf16x8 = __attribute__((ext_vector_type(8))) short;
using f32x4 = __attribute__((ext_vector_type(4))) float;
using u32x4 = __attribute__((ext_vector_type(4))) unsigned;
using u32x2 = __attribute__((ext_vector_type(2))) unsigned;
using i32x4 = __attribute__((ext_vector_type(4))) int;
typedef __bf16 bf16x2_t __attribute__((ext_vector_type(2)));
typedef float f32x2 __attribute__((ext_vector_type(2)));

#define DI __device__ __forceinline__
#define LAM __attribute__((always_inline))
DI void masked_copy8(float* d, const float* s, unsigned long long mask) {
    unsigned long long sv;
    asm volatile("s_and_saveexec_b64 %8, %17\n\ts_cbranch_execz .Lmc%=\n\t"
                 "v_mov_b32 %0, %9\n\tv_mov_b32 %1, %10\n\tv_mov_b32 %2, %11\n\tv_mov_b32 %3, %12\n\t"
                 "v_mov_b32 %4, %13\n\tv_mov_b32 %5, %14\n\tv_mov_b32 %6, %15\n\tv_mov_b32 %7, %16\n"
                 ".Lmc%=:\n\ts_mov_b64 exec, %8"
                 : "+v"(d[0]), "+v"(d[1]), "+v"(d[2]), "+v"(d[3]), "+v"(d[4]), "+v"(d[5]), "+v"(d[6]), "+v"(d[7]), "=&s"(sv)
                 : "v"(s[0]), "v"(s[1]), "v"(s[2]), "v"(s[3]), "v"(s[4]), "v"(s[5]), "v"(s[6]), "v"(s[7]), "s"(mask)
                 : "scc");
}
#define STAMP(i)
#define STAMP2(i)
#define STAMPX(i)

constexpr int D = 128;
constexpr int D3 = 384;

DI float bf_lo(unsigned u) { return __uint_as_float(u << 16); }
DI float bf_hi(unsigned u) { return __uint_as_float(u & 0xffff0000u); }
DI unsigned pack_bf16(float lo, float hi) {
    f32x2 v = {lo, hi};
    bf16x2_t b = __builtin_convertvector(v, bf16x2_t);
    return __builtin_bit_cast(unsigned, b);
}
DI float silu_f(float x) { return x * __builtin_amdgcn_rcpf(1.0f + __expf(-x)); }

struct Rec {
    unsigned key;
    float rh[3];
    float c[4];
};

typedef __attribute__((address_space(3))) void* lds_ptr_t;
typedef short s16x4 __attribute__((ext_vector_type(4)));
typedef __attribute__((address_space(3))) s16x4* lds_s16x4_ptr;

DI unsigned wave_incl_scan(unsigned v, int lane) {
    v += (unsigned)__builtin_amdgcn_update_dpp(0, (int)v, 0x111, 0xf, 0xf, false);
    v += (unsigned)__builtin_amdgcn_update_dpp(0, (int)v, 0x112, 0xf, 0xf, false);
    v += (unsigned)__builtin_amdgcn_update_dpp(0, (int)v, 0x114, 0xf, 0xf, false);
    v += (unsigned)__builtin_amdgcn_update_dpp(0, (int)v, 0x118, 0xf, 0xf, false);
    v += (unsigned)__builtin_amdgcn_update_dpp(0, (int)v, 0x142, 0xa, 0xf, false);
    v += (unsigned)__builtin_amdgcn_update_dpp(0, (int)v, 0x143, 0xc, 0xf, false);
    return v;
}
DI unsigned mbcnt64(unsigned long long m) {
    return __builtin_amdgcn_mbcnt_hi((unsigned)(m >> 32), __builtin_amdgcn_mbcnt_lo((unsigned)m, 0));
}
DI void dma16(const __amdgpu_buffer_rsrc_t rsrc, unsigned voff, unsigned lds_dst) {
    unsigned keep;
    asm volatile("s_mov_b32 %0, m0\n\ts_mov_b32 m0, %3\n\ts_nop 0\n\tbuffer_load_dwordx4 %1, %2, 0 offen lds\n\ts_mov_b32 m0, %0"
                 : "=&s"(keep) : "v"(voff), "s"(rsrc), "s"(lds_dst) : "memory");
}
#define GATHER_BITS "sc1"
DI void dma16g(const __amdgpu_buffer_rsrc_t rsrc, unsigned voff, unsigned lds_dst) {
    unsigned keep;
    asm volatile("s_mov_b32 %0, m0\n\ts_mov_b32 m0, %3\n\ts_nop 0\n\tbuffer_load_dwordx4 %1, %2, 0 offen " GATHER_BITS " lds\n\ts_mov_b32 m0, %0"
                 : "=&s"(keep) : "v"(voff), "s"(rsrc), "s"(lds_dst) : "memory");
}
DI unsigned lds_addr(const void* p) { return (unsigned)(size_t)(lds_ptr_t)p; }

constexpr int K1_THREADS = 512;
constexpr int G_TILES = 5;
constexpr int G_ROWS = 16 * G_TILES;
constexpr int HP = 272;
constexpr int GO_S = 131072;
constexpr int GO_H = 0;
constexpr int GO_OST = 32768;
constexpr int OP = 1040;
constexpr int K1_SMEM = 163840;
#define ST_AUX 16

DI unsigned wswz(unsigned row) { return ((row & 3u) << 2) | ((row >> 2) & 3u); }

DI void gemm_block(int blk, const unsigned short* __restrict__ sv, const unsigned short* __restrict__ w1,
                   const unsigned short* __restrict__ w2, unsigned char* __restrict__ TP, int N, char* smem) {
    const int tid = threadIdx.x;
    const int lane = tid & 63;
    const int w = __builtin_amdgcn_readfirstlane(tid >> 6);
    const int i16 = lane & 15, g = lane >> 4;
    const int row0 = blk * G_ROWS;
    const unsigned sm = lds_addr(smem);
    STAMP(0);
    const __amdgpu_buffer_rsrc_t rW1 = __builtin_amdgcn_make_buffer_rsrc((void*)w1, 0, D * D * 2, 0x00020000);
    const __amdgpu_buffer_rsrc_t rW2 = __builtin_amdgcn_make_buffer_rsrc((void*)w2, 0, D * D3 * 2, 0x00020000);
    const __amdgpu_buffer_rsrc_t rSV = __builtin_amdgcn_make_buffer_rsrc((void*)sv, 0, (unsigned)N * 1024u, 0x00020000);
    const unsigned rot = (unsigned)blk * 5u;

#pragma unroll
    for (int q = 0; q < 4; ++q) {
        const unsigned i = ((unsigned)(w + 8 * q) + rot) & 31u;
        const unsigned row = 4u * i + (unsigned)g;
        dma16(rW1, row * 256u + (((unsigned)i16 ^ wswz(row)) * 16u), sm + i * 1024u);
    }
#pragma unroll
    for (int q = 0; q < 3; ++q) {
        const unsigned i = (unsigned)(w + 8 * q);
        if (i < G_ROWS / 4) {
            const unsigned node = 4u * i + (unsigned)g;
            dma16(rSV, ((unsigned)row0 + node) * 1024u + (((unsigned)i16 ^ (node & 15u)) * 16u), sm + GO_S + i * 1024u);
        }
    }
#pragma unroll
    for (int q = 0; q < 12; ++q) {
        const unsigned i = ((unsigned)(w + 8 * (q & 3)) + rot) & 31u, seg = (unsigned)(q >> 2);
        const unsigned row = 4u * i + (unsigned)g;
        dma16(rW2, row * 768u + seg * 256u + (((unsigned)i16 ^ wswz(row)) * 16u), sm + 32768u * (seg + 1u) + i * 1024u);
    }
    asm volatile("s_waitcnt vmcnt(12)" ::: "memory");
    __syncthreads();
    STAMP(1);
    const unsigned tq = (unsigned)i16 >> 2, tp = (unsigned)lane & 3u;
    auto afrag = [&](int im, int ks) LAM {
        const unsigned row = 32u * ks + 8u * g + tq;
        const s16x4 lo = __builtin_amdgcn_ds_read_tr16_b64_v4i16((lds_s16x4_ptr)(smem + im * 32768 + 256u * row +
                            16u * ((2u * w + (tp >> 1)) ^ wswz(row)) + 8u * (tp & 1u)));
        const s16x4 hi = __builtin_amdgcn_ds_read_tr16_b64_v4i16((lds_s16x4_ptr)(smem + im * 32768 + 256u * (row + 4u) +
                            16u * ((2u * w + (tp >> 1)) ^ wswz(row + 4u)) + 8u * (tp & 1u)));
        return bf16x8{lo[0], lo[1], lo[2], lo[3], hi[0], hi[1], hi[2], hi[3]};
    };
    bf16x8 a1[4];
#pragma unroll
    for (int ks = 0; ks < 4; ++ks) a1[ks] = afrag(0, ks);
    __syncthreads();
#pragma unroll 1
    for (int t = 0; t < G_TILES; ++t) {
        const char* st = smem + GO_S + (16 * t + i16) * 256;
        f32x4 c = {0.f, 0.f, 0.f, 0.f};
#pragma unroll
        for (int ks = 0; ks < 4; ++ks) {
            const bf16x8 b = *reinterpret_cast<const bf16x8*>(st + (((unsigned)(4 * ks + g) ^ (unsigned)i16) * 16u));
            c = __builtin_amdgcn_mfma_f32_16x16x32_bf16(a1[ks], b, c, 0, 0, 0);
        }
        u32x2 o;
        o[0] = pack_bf16(silu_f(c[0]), silu_f(c[1]));
        o[1] = pack_bf16(silu_f(c[2]), silu_f(c[3]));
        *reinterpret_cast<u32x2*>(smem + GO_H + (16 * t + i16) * HP + (16 * w + 4 * g) * 2) = o;
    }
    STAMP(2);
    asm volatile("s_waitcnt vmcnt(0)" ::: "memory");
    __syncthreads();
    bf16x8 a2[3][4];
#pragma unroll
    for (int q = 0; q < 3; ++q)
#pragma unroll
        for (int ks = 0; ks < 4; ++ks) a2[q][ks] = afrag(q + 1, ks);
    __syncthreads();
    STAMP(3);
    const __amdgpu_buffer_rsrc_t rT = __builtin_amdgcn_make_buffer_rsrc((void*)TP, 0, (unsigned)N * 1024u, 0x00020000);
    const unsigned sw = ((((unsigned)(2 * w + (g >> 1))) ^ (unsigned)i16) * 16u) + (unsigned)(g & 1) * 8u;
    char* ost = smem + GO_OST;
#pragma unroll 1
    for (int t = 0; t < G_TILES; ++t) {
        char* orow = ost + (16 * t + i16) * OP + 32 * w + 8 * g;
        bf16x8 bh[4];
#pragma unroll
        for (int ks = 0; ks < 4; ++ks)
            bh[ks] = *reinterpret_cast<const bf16x8*>(smem + GO_H + (16 * t + i16) * HP + (32 * ks + 8 * g) * 2);
        *reinterpret_cast<u32x2*>(orow + 768) = *reinterpret_cast<const u32x2*>(smem + GO_S + (16 * t + i16) * 256 + sw);
        f32x4 c[3];
#pragma unroll
        for (int q = 0; q < 3; ++q) c[q] = f32x4{0.f, 0.f, 0.f, 0.f};
#pragma unroll
        for (int ks = 0; ks < 4; ++ks)
#pragma unroll
            for (int q = 0; q < 3; ++q) c[q] = __builtin_amdgcn_mfma_f32_16x16x32_bf16(a2[q][ks], bh[ks], c[q], 0, 0, 0);
#pragma unroll
        for (int q = 0; q < 3; ++q) {
            u32x2 o;
            o[0] = pack_bf16(c[q][0], c[q][1]);
            o[1] = pack_bf16(c[q][2], c[q][3]);
            *reinterpret_cast<u32x2*>(orow + q * 256) = o;
        }
    }
    STAMP(4);
    __syncthreads();
#pragma unroll 2
    for (int r = w; r < G_ROWS; r += 8) {
        const u32x4 o = *reinterpret_cast<const u32x4*>(ost + r * OP + lane * 16);
        __builtin_amdgcn_raw_buffer_store_b128(o, rT, (unsigned)(row0 + r) * 1024u + (unsigned)lane * 16u, 0, ST_AUX);
    }
    STAMP(5);
}

constexpr int EPT = 8;
constexpr int CH = K1_THREADS * EPT;
constexpr int MAXNB = 128;
constexpr int EO_POS = 0;
constexpr int POS_LDS_MAX = 98304;
constexpr int EO_SRC = POS_LDS_MAX;
constexpr int EO_DST = EO_SRC + CH * 4;
constexpr int EO_RANK = EO_DST + CH * 4;
constexpr int EO_HIST = EO_RANK + CH * 2;
static_assert(EO_HIST + 8 * MAXNB * 4 <= K1_SMEM, "LDS");

struct Pos3 { float x, y, z; };
DI Pos3 unpack_pos(unsigned a, unsigned b, unsigned odd16) {
    const unsigned lo = __builtin_amdgcn_alignbit(b, a, odd16);
    const unsigned hi = b >> odd16;
    return Pos3{bf_lo(lo), bf_hi(lo), bf_lo(hi)};
}
DI Pos3 pos_lds(const char* lp, unsigned n) {
    const unsigned off = 6u * n;
    const unsigned* p = reinterpret_cast<const unsigned*>(lp + (off & ~3u));
    return unpack_pos(p[0], p[1], (off & 2u) << 3);
}
DI Pos3 pos_glb(const unsigned char* __restrict__ posb, unsigned n, unsigned lim) {
    const unsigned off = 6u * n;
    const unsigned a = off < lim ? off : lim;
    unsigned long long v;
    __builtin_memcpy(&v, posb + a, 8);
    v >>= (off - a) * 8u;
    return Pos3{bf_lo((unsigned)v), bf_hi((unsigned)v), bf_lo((unsigned)(v >> 32))};
}
DI float sin_rev(float r) { return __builtin_amdgcn_sinf(r - __builtin_rintf(r)); }
DI float cos_rev(float r) { return __builtin_amdgcn_cosf(r - __builtin_rintf(r)); }

template <bool POS_LDS>
DI void edge_block(int chunk, int nchunks, const int* __restrict__ src, const int* __restrict__ dst,
                   const unsigned char* __restrict__ posb, const unsigned short* __restrict__ freqs,
                   Rec* __restrict__ rec, unsigned* __restrict__ tab, int N, int E,
                   int BN, int NB, unsigned binmagic, char* smem) {
    const int tid = threadIdx.x;
    const int lane = tid & 63;
    const int w = __builtin_amdgcn_readfirstlane(tid >> 6);
    unsigned* it_src = reinterpret_cast<unsigned*>(smem + EO_SRC);
    unsigned* it_dst = reinterpret_cast<unsigned*>(smem + EO_DST);
    unsigned short* it_rank = reinterpret_cast<unsigned short*>(smem + EO_RANK);
    unsigned* hist = reinterpret_cast<unsigned*>(smem + EO_HIST);
    const unsigned lim = 6u * (unsigned)N - 8u;
    STAMP(0);
    const int wbase = chunk * CH + w * (EPT * 64);
    int es[EPT], ed[EPT];
#pragma unroll
    for (int j = 0; j < EPT / 4; ++j) {
        const int e = wbase + (j * 64 + lane) * 4;
        if (e + 3 < E) {
            const i32x4 s4 = *reinterpret_cast<const i32x4*>(src + e);
            const i32x4 d4 = *reinterpret_cast<const i32x4*>(dst + e);
#pragma unroll
            for (int q = 0; q < 4; ++q) { es[4 * j + q] = s4[q]; ed[4 * j + q] = d4[q]; }
        } else {
#pragma unroll
            for (int q = 0; q < 4; ++q) {
                es[4 * j + q] = (e + q < E) ? src[e + q] : -1;
                ed[4 * j + q] = (e + q < E) ? dst[e + q] : 0;
            }
        }
    }
    if (POS_LDS) {
        const unsigned bytes = 6u * (unsigned)N;
        const __amdgpu_buffer_rsrc_t rP = __builtin_amdgcn_make_buffer_rsrc((void*)posb, 0, bytes & ~3u, 0x00020000);
        const unsigned sm = lds_addr(smem);
        for (unsigned o = (unsigned)w * 1024u; o < bytes; o += 8192u) dma16(rP, o + (unsigned)lane * 16u, sm + EO_POS + o);
    }
    hist[tid] = 0; hist[tid + K1_THREADS] = 0;
    if (POS_LDS) {
        asm volatile("s_waitcnt vmcnt(0)" ::: "memory");
        __syncthreads();
        if ((N & 1) && tid == 0)
            *reinterpret_cast<unsigned*>(smem + EO_POS + ((6u * (unsigned)N) & ~3u)) =
                (unsigned)*reinterpret_cast<const unsigned short*>(posb + 6u * (unsigned)N - 2u);
        if (N & 1) __syncthreads();
    } else {
        __syncthreads();
    }
    STAMP(1);
    int cnt = 0;
#pragma unroll
    for (int j = 0; j < EPT; ++j) {
        const unsigned s = (unsigned)(es[j] < 0 ? 0 : es[j]), d = (unsigned)ed[j];
        const Pos3 a = POS_LDS ? pos_lds(smem + EO_POS, s) : pos_glb(posb, s, lim);
        const Pos3 b = POS_LDS ? pos_lds(smem + EO_POS, d) : pos_glb(posb, d, lim);
        const float rx = a.x - b.x, ry = a.y - b.y, rz = a.z - b.z;
        const float d2 = rx * rx + ry * ry + rz * rz + 1e-8f;
        const bool act = (es[j] >= 0) && !(d2 >= 25.0f && d2 < __builtin_inff());
        const unsigned long long m = __ballot(act);
        if (act) {
            const int p = w * (EPT * 64) + cnt + (int)mbcnt64(m);
            it_src[p] = s;
            it_dst[p] = d;
        }
        cnt += __popcll(m);
    }
    STAMP(2);
    unsigned* myhist = hist + w * MAXNB;
#pragma unroll 1
    for (int b = lane; b < cnt; b += 64) {
        const int p = w * (EPT * 64) + b;
        it_rank[p] = (unsigned short)atomicAdd(&myhist[__umulhi(it_dst[p], binmagic)], 1u);
    }
    __syncthreads();
    if (w == 0) {
        u32x2 h[8];
        unsigned t0 = 0, t1 = 0;
#pragma unroll
        for (int ww = 0; ww < 8; ++ww) {
            h[ww] = *reinterpret_cast<const u32x2*>(hist + ww * MAXNB + 2 * lane);
            t0 += h[ww][0]; t1 += h[ww][1];
        }
        unsigned r0 = wave_incl_scan(t0 + t1, lane) - t0 - t1, r1 = r0 + t0;
        const int ncp = (nchunks + 3) & ~3;
        if (2 * lane < NB) tab[(size_t)(2 * lane) * ncp + chunk] = r0 | (t0 << 16);
        if (2 * lane + 1 < NB) tab[(size_t)(2 * lane + 1) * ncp + chunk] = r1 | (t1 << 16);
#pragma unroll
        for (int ww = 0; ww < 8; ++ww) {
            *reinterpret_cast<u32x2*>(hist + ww * MAXNB + 2 * lane) = u32x2{r0, r1};
            r0 += h[ww][0]; r1 += h[ww][1];
        }
    }
    __syncthreads();
    STAMP(3);
    float fr[4];
#pragma unroll
    for (int k = 0; k < 4; ++k) fr[k] = bf_lo((unsigned)freqs[k]) * 0.15915494309189535f;
#pragma unroll 1
    for (int b = lane; b < cnt; b += 64) {
        const int p = w * (EPT * 64) + b;
        const unsigned s = it_src[p], dd = it_dst[p];
        const Pos3 a = POS_LDS ? pos_lds(smem + EO_POS, s) : pos_glb(posb, s, lim);
        const Pos3 c = POS_LDS ? pos_lds(smem + EO_POS, dd) : pos_glb(posb, dd, lim);
        const float rx = a.x - c.x, ry = a.y - c.y, rz = a.z - c.z;
        const unsigned bin = __umulhi(dd, binmagic);
        const unsigned dl = dd - bin * (unsigned)BN;
        const unsigned pos = myhist[bin] + (unsigned)it_rank[p];
        const float d2 = rx * rx + ry * ry + rz * rz + 1e-8f;
        const float inv = __builtin_amdgcn_rsqf(d2);
        const float d = d2 * inv;
        float fc = 0.5f * (cos_rev(d * 0.1f) + 1.0f);
        fc = (d < 5.0f) ? fc : 0.0f * d;
        const float sc = inv * fc;
        u32x4 r0, r1;
        r0[0] = s | (dl << 22);
        r0[1] = __float_as_uint(rx * inv);
        r0[2] = __float_as_uint(ry * inv);
        r0[3] = __float_as_uint(rz * inv);
        r1[0] = __float_as_uint(sin_rev(d * fr[0]) * sc);
        r1[1] = __float_as_uint(sin_rev(d * fr[1]) * sc);
        r1[2] = __float_as_uint(sin_rev(d * fr[2]) * sc);
        r1[3] = __float_as_uint(sin_rev(d * fr[3]) * sc);
        u32x4* o = reinterpret_cast<u32x4*>(rec + (size_t)chunk * CH + pos);
        o[0] = r0;
        o[1] = r1;
    }
    STAMP(5);
}

__global__ __launch_bounds__(K1_THREADS) void painn_k1(const unsigned short* __restrict__ sv,
                                                       const unsigned char* __restrict__ posb,
                                                       const int* __restrict__ src, const int* __restrict__ dst,
                                                       const unsigned short* __restrict__ freqs,
                                                       const unsigned short* __restrict__ w1,
                                                       const unsigned short* __restrict__ w2,
                                                       unsigned char* __restrict__ TP, Rec* __restrict__ rec,
                                                       unsigned* __restrict__ tab, int N, int E, int BN, int NB,
                                                       int nchunks, int ngemm, unsigned binmagic) {
    extern __shared__ __attribute__((aligned(16))) char smem[];
    const int b = blockIdx.x;
    STAMPX(0);
    if (b < nchunks) {
        if (6 * N + 16 <= POS_LDS_MAX)
            edge_block<true>(b, nchunks, src, dst, posb, freqs, rec, tab, N, E, BN, NB, binmagic, smem);
        else
            edge_block<false>(b, nchunks, src, dst, posb, freqs, rec, tab, N, E, BN, NB, binmagic, smem);
    } else {
        gemm_block(b - nchunks, sv, w1, w2, TP, N, smem);
    }
}

constexpr int K2_NW = 16;
constexpr int K2_THREADS = K2_NW * 64;
constexpr int K2_BN = 100;
constexpr int K2_NG = K2_NW * 2;
constexpr int K2_CAP = 2048;
constexpr int K2_ICAP = K2_CAP + K2_BN + K2_NG + 20;
constexpr int K2_MAXCH = 256;
#define RING_R 2
constexpr int K2_R = RING_R;
constexpr int K2_SLOT = 1536;
constexpr int O_RING = 0;
constexpr int O_ITEMS = K2_NW * K2_R * K2_SLOT;
constexpr int O_PERM = O_ITEMS + (K2_CAP + 1) * 32;
constexpr int O_SKEY = O_PERM + K2_ICAP * 2;
constexpr int O_NOFF = O_SKEY + K2_ICAP * 4;
constexpr int O_HEAD = O_NOFF + 128 * 4;
constexpr int O_WSUM = O_HEAD + K2_NG * 4;
constexpr int O_DUMP = O_WSUM + 64;
constexpr int K2_SMEM = O_DUMP + 1024;
static_assert(K2_SMEM <= 163840, "LDS");
static_assert(K2_ICAP % 8 == 0, "align");
static_assert(K2_CAP <= 2 * K2_THREADS, "rounds");
constexpr unsigned SRC_MASK = 0x3fffffu;
constexpr unsigned F_FIN = 0x80000000u, F_SPLIT = 0x40000000u;
#define OUT_AUX 2

DI void masked_zero8(float* d, unsigned long long mask) {
    unsigned long long sv;
    asm volatile("s_and_saveexec_b64 %8, %9\n\ts_cbranch_execz .Lmy%=\n\t"
                 "v_mov_b32 %0, 0\n\tv_mov_b32 %1, 0\n\tv_mov_b32 %2, 0\n\tv_mov_b32 %3, 0\n\t"
                 "v_mov_b32 %4, 0\n\tv_mov_b32 %5, 0\n\tv_mov_b32 %6, 0\n\tv_mov_b32 %7, 0\n"
                 ".Lmy%=:\n\ts_mov_b64 exec, %8"
                 : "+v"(d[0]), "+v"(d[1]), "+v"(d[2]), "+v"(d[3]), "+v"(d[4]), "+v"(d[5]), "+v"(d[6]), "+v"(d[7]), "=&s"(sv)
                 : "s"(mask) : "scc");
}

DI void k2_generic(const unsigned char* __restrict__ svb, const unsigned char* __restrict__ TP,
                   const unsigned short* __restrict__ wrbf, const Rec* __restrict__ rec, const unsigned* __restrict__ tab,
                   unsigned char* __restrict__ outb, int bin, int hf, int node0, int nn, int nchunks) {
    const int lane = threadIdx.x & 63;
    const int w = __builtin_amdgcn_readfirstlane(threadIdx.x >> 6);
    const int gC = lane >> 5, l32 = lane & 31;
    const int ncp = (nchunks + 3) & ~3;
    const size_t co = (size_t)(hf * 128 + l32 * 4);
    float wr[3][4][2];
#pragma unroll
    for (int sg = 0; sg < 3; ++sg)
#pragma unroll
        for (int k = 0; k < 4; ++k) {
            const unsigned u = *reinterpret_cast<const unsigned*>(wrbf + k * D3 + sg * D + 64 * hf + 2 * l32);
            wr[sg][k][0] = bf_lo(u); wr[sg][k][1] = bf_hi(u);
        }
#pragma unroll 1
    for (int j = w; j < nn; j += K2_NW) {
        float acc[8];
#pragma unroll
        for (int r = 0; r < 8; ++r) acc[r] = 0.f;
#pragma unroll 1
        for (int c = 0; c < nchunks; ++c) {
            const unsigned ent = tab[(size_t)bin * ncp + c];
            const int cnt = (int)(ent >> 16);
            const Rec* rp = rec + (size_t)c * CH + (ent & 0xffffu);
#pragma unroll 1
            for (int b = 0; b < cnt; b += 64) {
                const bool in = b + lane < cnt;
                const unsigned key = in ? rp[b + lane].key : 0u;
                unsigned long long m = __ballot(in && (key >> 22) == (unsigned)j);
#pragma unroll 1
                while (m) {
                    const int i0 = __builtin_ctzll(m);
                    m &= m - 1;
                    const int i1 = m ? __builtin_ctzll(m) : -1;
                    m &= m - 1;
                    const int im = gC ? i1 : i0;
                    if (im >= 0) {
                        const u32x4 rr = reinterpret_cast<const u32x4*>(rp + b + im)[0], kk = reinterpret_cast<const u32x4*>(rp + b + im)[1];
                        const size_t n = rr[0] & SRC_MASK;
                        const unsigned* pp = reinterpret_cast<const unsigned*>(TP + n * 1024 + co);
                        const unsigned* vp = reinterpret_cast<const unsigned*>(svb + n * 1024 + co);
                        const unsigned ps = pp[0], pv = pp[64], pr = pp[128], vx = vp[64], vy = vp[128], vz = vp[192];
#pragma unroll
                        for (int q = 0; q < 2; ++q) {
                            float wf[3];
#pragma unroll
                            for (int sg = 0; sg < 3; ++sg)
                                wf[sg] = __uint_as_float(kk[0]) * wr[sg][0][q] + __uint_as_float(kk[1]) * wr[sg][1][q] +
                                         __uint_as_float(kk[2]) * wr[sg][2][q] + __uint_as_float(kk[3]) * wr[sg][3][q];
                            const float mv = (q ? bf_hi(pv) : bf_lo(pv)) * wf[1], mr = (q ? bf_hi(pr) : bf_lo(pr)) * wf[2];
                            acc[q] += (q ? bf_hi(ps) : bf_lo(ps)) * wf[0];
                            acc[2 + q] += mv * (q ? bf_hi(vx) : bf_lo(vx)) + mr * __uint_as_float(rr[1]);
                            acc[4 + q] += mv * (q ? bf_hi(vy) : bf_lo(vy)) + mr * __uint_as_float(rr[2]);
                            acc[6 + q] += mv * (q ? bf_hi(vz) : bf_lo(vz)) + mr * __uint_as_float(rr[3]);
                        }
                    }
                }
            }
        }
#pragma unroll
        for (int r = 0; r < 8; ++r) acc[r] += __shfl_xor(acc[r], 32, 64);
        if (gC == 0) {
            const size_t n = (size_t)(node0 + j);
            const unsigned* bp = reinterpret_cast<const unsigned*>(svb + n * 1024 + co);
            unsigned* op = reinterpret_cast<unsigned*>(outb + n * 1024 + co);
#pragma unroll
            for (int r = 0; r < 4; ++r) {
                const unsigned b = bp[64 * r];
                op[64 * r] = pack_bf16(bf_lo(b) + acc[2 * r], bf_hi(b) + acc[2 * r + 1]);
            }
        }
    }
}

__global__ __launch_bounds__(K2_THREADS) void painn_k2(const unsigned char* __restrict__ svb,
                                                       const unsigned char* __restrict__ TP,
                                                       const unsigned short* __restrict__ wrbf,
                                                       const Rec* __restrict__ rec,
                                                       const unsigned* __restrict__ tab,
                                                       unsigned char* __restrict__ outb, int N, int BN, int NB, int nchunks) {
    extern __shared__ __attribute__((aligned(16))) char smem[];
    const int tid = threadIdx.x;
    const int lane = tid & 63;
    const int w = __builtin_amdgcn_readfirstlane(tid >> 6);
    const int hf = blockIdx.x & 1;
    const int bin = blockIdx.x >> 1;
    STAMP2(0); STAMPX(2048);
    const int node0 = bin * BN;
    const int nn = min(BN, N - node0);
    unsigned* hist = reinterpret_cast<unsigned*>(smem + O_RING);
    u32x4* items = reinterpret_cast<u32x4*>(smem + O_ITEMS);
    unsigned short* perm = reinterpret_cast<unsigned short*>(smem + O_PERM);
    unsigned* skey = reinterpret_cast<unsigned*>(smem + O_SKEY);
    unsigned* noff = reinterpret_cast<unsigned*>(smem + O_NOFF);
    unsigned* headinfo = reinterpret_cast<unsigned*>(smem + O_HEAD);
    unsigned* wsum = reinterpret_cast<unsigned*>(smem + O_WSUM);

    const __amdgpu_buffer_rsrc_t rTP = __builtin_amdgcn_make_buffer_rsrc((void*)TP, 0, (unsigned)N * 1024u, 0x00020000);
    const __amdgpu_buffer_rsrc_t rSV = __builtin_amdgcn_make_buffer_rsrc((void*)svb, 0, (unsigned)N * 1024u, 0x00020000);
    const __amdgpu_buffer_rsrc_t rOut = __builtin_amdgcn_make_buffer_rsrc((void*)outb, 0, (unsigned)N * 1024u, 0x00020000);
    const __amdgpu_buffer_rsrc_t rRec =
        __builtin_amdgcn_make_buffer_rsrc((void*)rec, 0, (unsigned)nchunks * CH * 32u, 0x00020000);

    const int ncp = (nchunks + 3) & ~3;
    u32x4 e = {0u, 0u, 0u, 0u};
    if (4 * lane < nchunks) e = *reinterpret_cast<const u32x4*>(tab + (size_t)bin * ncp + 4 * lane);
#pragma unroll
    for (int q = 1; q < 4; ++q) e[q] = 4 * lane + q < nchunks ? e[q] : 0u;
    unsigned wpk[3][4];
#pragma unroll
    for (int sg = 0; sg < 3; ++sg)
#pragma unroll
        for (int k = 0; k < 4; ++k)
            wpk[sg][k] = *reinterpret_cast<const unsigned*>(wrbf + k * D3 + sg * D + 64 * hf + 2 * (lane & 31));
    if (tid < 16 * K2_BN / 4) reinterpret_cast<u32x4*>(hist)[tid] = u32x4{0u, 0u, 0u, 0u};
    if (tid < 2) items[2 * K2_CAP + tid] = u32x4{0u, 0u, 0u, 0u};
    if (tid < K2_NG) headinfo[tid] = 0xffffffffu;
    STAMP2(1);
    int T;
    {
        const unsigned c0 = e[0] >> 16, c1 = e[1] >> 16, c2 = e[2] >> 16, c3 = e[3] >> 16;
        const unsigned incl = wave_incl_scan(c0 + c1 + c2 + c3, lane);
        T = __builtin_amdgcn_readlane((int)incl, 63);
        const unsigned o3 = incl - c3, o2 = o3 - c2, o1 = o2 - c1, o0 = o1 - c0;
        const unsigned me = (w & 3) == 0 ? e[0] : (w & 3) == 1 ? e[1] : (w & 3) == 2 ? e[2] : e[3];
        const unsigned mo = (w & 3) == 0 ? o0 : (w & 3) == 1 ? o1 : (w & 3) == 2 ? o2 : o3;
        const unsigned ib = lds_addr(items);
        if (T <= K2_CAP && nchunks <= K2_MAXCH && BN <= K2_BN) {
#pragma unroll 1
            for (int l = w >> 2; 4 * l + (w & 3) < nchunks; l += 4) {
                const unsigned ee = (unsigned)__builtin_amdgcn_readlane((int)me, l);
                const unsigned o = (unsigned)__builtin_amdgcn_readlane((int)mo, l);
                const unsigned cnt2 = 2u * (ee >> 16), gb = ((unsigned)(4 * l + (w & 3)) * CH + (ee & 0xffffu)) * 32u;
#pragma unroll 1
                for (unsigned i = 0; i < cnt2; i += 64u)
                    if (i + (unsigned)lane < cnt2) dma16(rRec, gb + (i + (unsigned)lane) * 16u, ib + o * 32u + i * 16u);
            }
        }
    }
    if (T > K2_CAP || nchunks > K2_MAXCH || BN > K2_BN) {
        asm volatile("s_waitcnt vmcnt(0)" ::: "memory");
        k2_generic(svb, TP, wrbf, rec, tab, outb, bin, hf, node0, nn, nchunks);
        return;
    }
    const int Tc = T;
    const int M = Tc + nn;
    const int L = (M + K2_NG - 1) / K2_NG;
    asm volatile("s_waitcnt vmcnt(0)" ::: "memory");
    __syncthreads();
    STAMP2(2);
    unsigned* myhist = hist + w * K2_BN;
    unsigned rdk[2] = {0u, 0u}, rkey[2] = {0u, 0u};
#pragma unroll
    for (int r = 0; r < 2; ++r)
        if (K2_THREADS * r + tid < Tc) {
            rkey[r] = items[2 * (K2_THREADS * r + tid)][0];
            const unsigned dl = rkey[r] >> 22;
            rdk[r] = dl | (atomicAdd(&myhist[dl], 1u) << 16);
        }
    __syncthreads();
    STAMP2(4);
    if (w < 2) {
        unsigned ntot = 0;
        if (tid < K2_BN) {
            unsigned h[16];
#pragma unroll
            for (int ww = 0; ww < 16; ++ww) h[ww] = hist[ww * K2_BN + tid];
#pragma unroll
            for (int ww = 0; ww < 16; ++ww) { hist[ww * K2_BN + tid] = ntot; ntot += h[ww]; }
        }
        const unsigned len = tid < nn ? ntot + 1u : 0u;
        const unsigned nincl = wave_incl_scan(len, lane);
        if (lane == 63) wsum[w] = nincl;
        noff[tid] = (nincl - len) | (ntot << 16);
    }
    __syncthreads();
    STAMP2(5);
    const unsigned w0tot = wsum[0];
    const unsigned Mdiv = 0xffffffffu / (unsigned)L + 1u;
#pragma unroll
    for (int r = 0; r < 2; ++r)
        if (K2_THREADS * r + tid < Tc) {
            const unsigned dl = rdk[r] & 0xffffu;
            const unsigned p = (noff[dl] & 0xffffu) + (dl >= 64u ? w0tot : 0u) + myhist[dl] + (rdk[r] >> 16);
            perm[p] = (unsigned short)(K2_THREADS * r + tid);
            skey[p] = rkey[r] & SRC_MASK;
        }
    if (tid < nn) {
        const unsigned no = noff[tid];
        const unsigned p0 = (no & 0xffffu) + (tid >= 64 ? w0tot : 0u), pf = p0 + (no >> 16);
        const unsigned g0 = L == 1 ? p0 : __umulhi(p0, Mdiv), gf = L == 1 ? pf : __umulhi(pf, Mdiv);
        perm[pf] = (unsigned short)K2_CAP;
        skey[pf] = (unsigned)(node0 + tid) | F_FIN | (g0 < gf ? F_SPLIT : 0u);
        if (g0 < gf) headinfo[gf] = g0;
    }
    if (tid >= 512 && M + (tid - 512) < K2_NG * L + 16) {
        perm[M + tid - 512] = (unsigned short)K2_CAP;
        skey[M + tid - 512] = 0u;
    }
    float wr[3][4][2];
#pragma unroll
    for (int sg = 0; sg < 3; ++sg)
#pragma unroll
        for (int k = 0; k < 4; ++k) { wr[sg][k][0] = bf_lo(wpk[sg][k]); wr[sg][k][1] = bf_hi(wpk[sg][k]); }
    __syncthreads();
    STAMP2(9);

    float acc[8], accH[8];
#pragma unroll
    for (int r = 0; r < 8; ++r) { acc[r] = 0.f; accH[r] = 0.f; }
    unsigned hnode = 0xffffffffu;
    const int gC = lane >> 5, l32 = lane & 31;
    const int gD = lane >= 24 ? 1 : 0, lD = lane - 24 * gD;
    const unsigned short* pp = perm + (2 * w + gC) * L;
    const unsigned* pkC = skey + (2 * w + gC) * L;
    const unsigned* pkD = skey + (2 * w + gD) * L;
    const unsigned offP = (unsigned)((lD >> 3) * 256 + hf * 128 + (lD & 7) * 16);
    const unsigned offS = lD < 8 ? (unsigned)(768 + hf * 128 + lD * 16) : 0x80000000u;
    const unsigned offV = offP + 256u;
    const unsigned ringb = lds_addr(smem + O_RING + w * (K2_R * K2_SLOT));
    const char* ringp = smem + O_RING + w * (K2_R * K2_SLOT) + gC * 384 + l32 * 4;

    auto issue = [&](unsigned key, int slot) LAM {
        const unsigned ro = (key & SRC_MASK) * 1024u;
        if (lane < 48) {
            dma16g(rTP, ro + ((key & F_FIN) ? offS : offP), ringb + (unsigned)slot * K2_SLOT);
            dma16g(rSV, ro + offV, ringb + (unsigned)slot * K2_SLOT + 768u);
        }
    };
    auto compute = [&](int slot, unsigned q, unsigned key) LAM {
        const u32x4 rr = items[2 * q], kk = items[2 * q + 1];
        const unsigned* dp = reinterpret_cast<const unsigned*>(ringp + slot * K2_SLOT);
        asm volatile("s_waitcnt vmcnt(%0)" :: "n"(2 * (K2_R - 1)) : "memory");
        const unsigned ps = dp[0], pv = dp[32], pr = dp[64], vx = dp[192], vy = dp[224], vz = dp[256];
        const float hx = __uint_as_float(rr[1]), hy = __uint_as_float(rr[2]), hz = __uint_as_float(rr[3]);
        const float c0 = __uint_as_float(kk[0]), c1 = __uint_as_float(kk[1]), c2 = __uint_as_float(kk[2]), c3 = __uint_as_float(kk[3]);
        float u[8];
#pragma unroll
        for (int j = 0; j < 2; ++j) {
            u[j] = j ? bf_hi(ps) : bf_lo(ps);
            u[2 + j] = j ? bf_hi(vx) : bf_lo(vx);
            u[4 + j] = j ? bf_hi(vy) : bf_lo(vy);
            u[6 + j] = j ? bf_hi(vz) : bf_lo(vz);
            const float fv = j ? bf_hi(pv) : bf_lo(pv), fr = j ? bf_hi(pr) : bf_lo(pr);
            float wfs, wfv, wfr;
            asm("v_mul_f32 %0, %3, %7\n\tv_mul_f32 %1, %3, %11\n\tv_mul_f32 %2, %3, %15\n\t"
                "v_fmac_f32 %0, %4, %8\n\tv_fmac_f32 %1, %4, %12\n\tv_fmac_f32 %2, %4, %16\n\t"
                "v_fmac_f32 %0, %5, %9\n\tv_fmac_f32 %1, %5, %13\n\tv_fmac_f32 %2, %5, %17\n\t"
                "v_fmac_f32 %0, %6, %10\n\tv_fmac_f32 %1, %6, %14\n\tv_fmac_f32 %2, %6, %18\n\t"
                "v_mul_f32 %1, %19, %1\n\tv_mul_f32 %2, %20, %2"
                : "=&v"(wfs), "=&v"(wfv), "=&v"(wfr)
                : "v"(c0), "v"(c1), "v"(c2), "v"(c3), "v"(wr[0][0][j]), "v"(wr[0][1][j]), "v"(wr[0][2][j]), "v"(wr[0][3][j]),
                  "v"(wr[1][0][j]), "v"(wr[1][1][j]), "v"(wr[1][2][j]), "v"(wr[1][3][j]),
                  "v"(wr[2][0][j]), "v"(wr[2][1][j]), "v"(wr[2][2][j]), "v"(wr[2][3][j]), "v"(fv), "v"(fr));
            asm("v_fmac_f32 %0, %4, %5\n\tv_fmac_f32 %1, %6, %8\n\tv_fmac_f32 %2, %6, %9\n\tv_fmac_f32 %3, %6, %10\n\t"
                "v_fmac_f32 %1, %7, %11\n\tv_fmac_f32 %2, %7, %12\n\tv_fmac_f32 %3, %7, %13"
                : "+v"(acc[j]), "+v"(acc[2 + j]), "+v"(acc[4 + j]), "+v"(acc[6 + j])
                : "v"(u[j]), "v"(wfs), "v"(wfv), "v"(wfr), "v"(u[2 + j]), "v"(u[4 + j]), "v"(u[6 + j]), "v"(hx), "v"(hy), "v"(hz));
        }
        const bool fin = (int)key < 0;
        if (__ballot(fin) != 0ull) {
            float t[8];
#pragma unroll
            for (int r = 0; r < 8; ++r) t[r] = acc[r] + u[r];
            const bool split = (key & F_SPLIT) != 0u;
            const unsigned oo = (fin && !split) ? (key & SRC_MASK) * 1024u + (unsigned)(hf * 128 + l32 * 4) : 0x80000000u;
#pragma unroll
            for (int r = 0; r < 4; ++r)
                __builtin_amdgcn_raw_buffer_store_b32(pack_bf16(t[2 * r], t[2 * r + 1]), rOut, oo + (unsigned)r * 256u, 0, OUT_AUX);
            masked_copy8(accH, t, __ballot(fin && split));
            hnode = (fin && split) ? (key & SRC_MASK) : hnode;
            masked_zero8(acc, __ballot(fin));
        }
    };

    {
#pragma unroll
        for (int k = 0; k < K2_R - 1; ++k) issue(pkD[k], k);
        unsigned kD = pkD[K2_R - 1], q = pp[0], kC = pkC[0];
        int slot = 0, fslot = K2_R - 1;
#pragma unroll 1
        for (int it = 0; it < L; ++it) {
            const unsigned kDn = pkD[it + K2_R], qn = pp[it + 1], kCn = pkC[it + 1];
            issue(kD, fslot);
            compute(slot, q, kC);
            fslot = slot;
            slot = slot + 1 == K2_R ? 0 : slot + 1;
            kD = kDn; q = qn; kC = kCn;
        }
        asm volatile("s_waitcnt vmcnt(0)" ::: "memory");
    }
    STAMP2(10);
    __syncthreads();
    STAMP2(11);
    {
        float* tl = reinterpret_cast<float*>(smem + O_RING);
        const int G = 2 * w + gC;
#pragma unroll
        for (int r = 0; r < 8; ++r) tl[(G * 8 + r) * 32 + l32] = acc[r];
        __syncthreads();
        const unsigned g0 = headinfo[G];
        if (g0 != 0xffffffffu) {
            float t[8];
#pragma unroll
            for (int r = 0; r < 8; ++r) t[r] = tl[(g0 * 8 + r) * 32 + l32];
#pragma unroll 1
            for (int gg = (int)g0 + 1; gg < G; ++gg) {
#pragma unroll
                for (int r = 0; r < 8; ++r) t[r] += tl[(gg * 8 + r) * 32 + l32];
            }
            const unsigned oo = hnode * 1024u + (unsigned)(hf * 128 + l32 * 4);
#pragma unroll
            for (int r = 0; r < 4; ++r)
                __builtin_amdgcn_raw_buffer_store_b32(pack_bf16(t[2 * r] + accH[2 * r], t[2 * r + 1] + accH[2 * r + 1]), rOut,
                                                      oo + (unsigned)r * 256u, 0, OUT_AUX);
        }
    }
    STAMP2(13);
}

extern "C" void kernel_launch(void* const* d_in, const int* in_sizes, int n_in, void* d_out, int out_size,
                              void* d_ws, size_t ws_size, hipStream_t stream) {
    const unsigned short* sv = (const unsigned short*)d_in[0];
    const unsigned char* pos = (const unsigned char*)d_in[1];
    const int* src = (const int*)d_in[2];
    const int* dst = (const int*)d_in[3];
    const unsigned short* freqs = (const unsigned short*)d_in[4];
    const unsigned short* w1 = (const unsigned short*)d_in[5];
    const unsigned short* w2 = (const unsigned short*)d_in[6];
    const unsigned short* wrbf = (const unsigned short*)d_in[7];
    const int N = in_sizes[0] / (4 * D);
    const int E = in_sizes[2];

    const int BN = (N + MAXNB - 1) / MAXNB;
    const int NB = (N + BN - 1) / BN;
    const int nchunks = (E + CH - 1) / CH;
    const int ngemm = (N + G_ROWS - 1) / G_ROWS;
    const unsigned binmagic = (unsigned)(0xffffffffull / (unsigned)BN + 1ull);

    char* ws = (char*)d_ws;
    unsigned char* TP = (unsigned char*)ws;
    size_t off = ((size_t)N * 1024 + 255) & ~(size_t)255;
    Rec* rec = (Rec*)(ws + off);
    off += (size_t)nchunks * CH * sizeof(Rec);
    unsigned* tab = (unsigned*)(ws + off);

    (void)hipFuncSetAttribute((const void*)painn_k1, hipFuncAttributeMaxDynamicSharedMemorySize, K1_SMEM);
    painn_k1<<<ngemm + nchunks, K1_THREADS, K1_SMEM, stream>>>(sv, pos, src, dst, freqs, w1, w2, TP, rec, tab, N, E,
                                                                BN, NB, nchunks, ngemm, binmagic);
    (void)hipFuncSetAttribute((const void*)painn_k2, hipFuncAttributeMaxDynamicSharedMemorySize, K2_SMEM);
    painn_k2<<<NB * 2, K2_THREADS, K2_SMEM, stream>>>((const unsigned char*)sv, TP, wrbf, rec, tab, (unsigned char*)d_out, N, BN, NB,
                                                      nchunks);
}
```

```cpp
#include <hip/hip_runtime.h>
#include <hip/hip_bf16.h>
#include <stdint.h>

using bf16x8 = __attribute__((ext_vector_type(8))) short;
using f32x4 = __attribute__((ext_vector_type(4))) float;
using u32x4 = __attribute__((ext_vector_type(4))) unsigned;
using u32x2 = __attribute__((ext_vector_type(2))) unsigned;
using i32x4 = __attribute__((ext_vector_type(4))) int;
typedef __bf16 bf16x2_t __attribute__((ext_vector_type(2)));
typedef float f32x2 __attribute__((ext_vector_type(2)));

#define DI __device__ __forceinline__
#define LAM __attribute__((always_inline))
DI void masked_copy8(float* d, const float* s, unsigned long long mask) {
    unsigned long long sv;
    asm volatile("s_and_saveexec_b64 %8, %17\n\ts_cbranch_execz .Lmc%=\n\t"
                 "v_mov_b32 %0, %9\n\tv_mov_b32 %1, %10\n\tv_mov_b32 %2, %11\n\tv_mov_b32 %3, %12\n\t"
                 "v_mov_b32 %4, %13\n\tv_mov_b32 %5, %14\n\tv_mov_b32 %6, %15\n\tv_mov_b32 %7, %16\n"
                 ".Lmc%=:\n\ts_mov_b64 exec, %8"
                 : "+v"(d[0]), "+v"(d[1]), "+v"(d[2]), "+v"(d[3]), "+v"(d[4]), "+v"(d[5]), "+v"(d[6]), "+v"(d[7]), "=&s"(sv)
                 : "v"(s[0]), "v"(s[1]), "v"(s[2]), "v"(s[3]), "v"(s[4]), "v"(s[5]), "v"(s[6]), "v"(s[7]), "s"(mask)
                 : "scc");
}
#define STAMP(i)
#define STAMP2(i)
#define STAMPX(i)

constexpr int D = 128;
constexpr int D3 = 384;

DI float bf_lo(unsigned u) { return __uint_as_float(u << 16); }
DI float bf_hi(unsigned u) { return __uint_as_float(u & 0xffff0000u); }
DI unsigned pack_bf16(float lo, float hi) {
    f32x2 v = {lo, hi};
    bf16x2_t b = __builtin_convertvector(v, bf16x2_t);
    return __builtin_bit_cast(unsigned, b);
}
DI float silu_f(float x) { return x * __builtin_amdgcn_rcpf(1.0f + __expf(-x)); }

struct Rec {
    unsigned key;
    float rh[3];
    float c[4];
};

typedef __attribute__((address_space(3))) void* lds_ptr_t;
typedef short s16x4 __attribute__((ext_vector_type(4)));
typedef __attribute__((address_space(3))) s16x4* lds_s16x4_ptr;

DI unsigned wave_incl_scan(unsigned v, int lane) {
    v += (unsigned)__builtin_amdgcn_update_dpp(0, (int)v, 0x111, 0xf, 0xf, false);
    v += (unsigned)__builtin_amdgcn_update_dpp(0, (int)v, 0x112, 0xf, 0xf, false);
    v += (unsigned)__builtin_amdgcn_update_dpp(0, (int)v, 0x114, 0xf, 0xf, false);
    v += (unsigned)__builtin_amdgcn_update_dpp(0, (int)v, 0x118, 0xf, 0xf, false);
    v += (unsigned)__builtin_amdgcn_update_dpp(0, (int)v, 0x142, 0xa, 0xf, false);
    v += (unsigned)__builtin_amdgcn_update_dpp(0, (int)v, 0x143, 0xc, 0xf, false);
    return v;
}
DI unsigned mbcnt64(unsigned long long m) {
    return __builtin_amdgcn_mbcnt_hi((unsigned)(m >> 32), __builtin_amdgcn_mbcnt_lo((unsigned)m, 0));
}
DI void dma16(const __amdgpu_buffer_rsrc_t rsrc, unsigned voff, unsigned lds_dst) {
    unsigned keep;
    asm volatile("s_mov_b32 %0, m0\n\ts_mov_b32 m0, %3\n\ts_nop 0\n\tbuffer_load_dwordx4 %1, %2, 0 offen lds\n\ts_mov_b32 m0, %0"
                 : "=&s"(keep) : "v"(voff), "s"(rsrc), "s"(lds_dst) : "memory");
}
#define GATHER_BITS "sc1"
DI void dma16g(const __amdgpu_buffer_rsrc_t rsrc, unsigned voff, unsigned lds_dst) {
    unsigned keep;
    asm volatile("s_mov_b32 %0, m0\n\ts_mov_b32 m0, %3\n\ts_nop 0\n\tbuffer_load_dwordx4 %1, %2, 0 offen " GATHER_BITS " lds\n\ts_mov_b32 m0, %0"
                 : "=&s"(keep) : "v"(voff), "s"(rsrc), "s"(lds_dst) : "memory");
}
DI unsigned lds_addr(const void* p) { return (unsigned)(size_t)(lds_ptr_t)p; }

constexpr int K1_THREADS = 512;
constexpr int G_TILES = 5;
constexpr int G_ROWS = 16 * G_TILES;
constexpr int HP = 272;
constexpr int GO_S = 131072;
constexpr int GO_H = 0;
constexpr int GO_OST = 32768;
constexpr int OP = 1040;
constexpr int K1_SMEM = 163840;
#define ST_AUX 16

DI unsigned wswz(unsigned row) { return ((row & 3u) << 2) | ((row >> 2) & 3u); }

DI void gemm_block(int blk, const unsigned short* __restrict__ sv, const unsigned short* __restrict__ w1,
                   const unsigned short* __restrict__ w2, unsigned char* __restrict__ TP, int N, char* smem) {
    const int tid = threadIdx.x;
    const int lane = tid & 63;
    const int w = __builtin_amdgcn_readfirstlane(tid >> 6);
    const int i16 = lane & 15, g = lane >> 4;
    const int row0 = blk * G_ROWS;
    const unsigned sm = lds_addr(smem);
    STAMP(0);
    const __amdgpu_buffer_rsrc_t rW1 = __builtin_amdgcn_make_buffer_rsrc((void*)w1, 0, D * D * 2, 0x00020000);
    const __amdgpu_buffer_rsrc_t rW2 = __builtin_amdgcn_make_buffer_rsrc((void*)w2, 0, D * D3 * 2, 0x00020000);
    const __amdgpu_buffer_rsrc_t rSV = __builtin_amdgcn_make_buffer_rsrc((void*)sv, 0, (unsigned)N * 1024u, 0x00020000);
    const unsigned rot = (unsigned)blk * 5u;

#pragma unroll
    for (int q = 0; q < 4; ++q) {
        const unsigned i = ((unsigned)(w + 8 * q) + rot) & 31u;
        const unsigned row = 4u * i + (unsigned)g;
        dma16(rW1, row * 256u + (((unsigned)i16 ^ wswz(row)) * 16u), sm + i * 1024u);
    }
#pragma unroll
    for (int q = 0; q < 3; ++q) {
        const unsigned i = (unsigned)(w + 8 * q);
        if (i < G_ROWS / 4) {
            const unsigned node = 4u * i + (unsigned)g;
            dma16(rSV, ((unsigned)row0 + node) * 1024u + (((unsigned)i16 ^ (node & 15u)) * 16u), sm + GO_S + i * 1024u);
        }
    }
#pragma unroll
    for (int q = 0; q < 12; ++q) {
        const unsigned i = ((unsigned)(w + 8 * (q & 3)) + rot) & 31u, seg = (unsigned)(q >> 2);
        const unsigned row = 4u * i + (unsigned)g;
        dma16(rW2, row * 768u + seg * 256u + (((unsigned)i16 ^ wswz(row)) * 16u), sm + 32768u * (seg + 1u) + i * 1024u);
    }
    asm volatile("s_waitcnt vmcnt(12)" ::: "memory");
    __syncthreads();
    STAMP(1);
    const unsigned tq = (unsigned)i16 >> 2, tp = (unsigned)lane & 3u;
    auto afrag = [&](int im, int ks) LAM {
        const unsigned row = 32u * ks + 8u * g + tq;
        const s16x4 lo = __builtin_amdgcn_ds_read_tr16_b64_v4i16((lds_s16x4_ptr)(smem + im * 32768 + 256u * row +
                            16u * ((2u * w + (tp >> 1)) ^ wswz(row)) + 8u * (tp & 1u)));
        const s16x4 hi = __builtin_amdgcn_ds_read_tr16_b64_v4i16((lds_s16x4_ptr)(smem + im * 32768 + 256u * (row + 4u) +
                            16u * ((2u * w + (tp >> 1)) ^ wswz(row + 4u)) + 8u * (tp & 1u)));
        return bf16x8{lo[0], lo[1], lo[2], lo[3], hi[0], hi[1], hi[2], hi[3]};
    };
    bf16x8 a1[4];
#pragma unroll
    for (int ks = 0; ks < 4; ++ks) a1[ks] = afrag(0, ks);
    __syncthreads();
#pragma unroll 1
    for (int t = 0; t < G_TILES; ++t) {
        const char* st = smem + GO_S + (16 * t + i16) * 256;
        f32x4 c = {0.f, 0.f, 0.f, 0.f};
#pragma unroll
        for (int ks = 0; ks < 4; ++ks) {
            const bf16x8 b = *reinterpret_cast<const bf16x8*>(st + (((unsigned)(4 * ks + g) ^ (unsigned)i16) * 16u));
            c = __builtin_amdgcn_mfma_f32_16x16x32_bf16(a1[ks], b, c, 0, 0, 0);
        }
        u32x2 o;
        o[0] = pack_bf16(silu_f(c[0]), silu_f(c[1]));
        o[1] = pack_bf16(silu_f(c[2]), silu_f(c[3]));
        *reinterpret_cast<u32x2*>(smem + GO_H + (16 * t + i16) * HP + (16 * w + 4 * g) * 2) = o;
    }
    STAMP(2);
    asm volatile("s_waitcnt vmcnt(0)" ::: "memory");
    __syncthreads();
    bf16x8 a2[3][4];
#pragma unroll
    for (int q = 0; q < 3; ++q)
#pragma unroll
        for (int ks = 0; ks < 4; ++ks) a2[q][ks] = afrag(q + 1, ks);
    __syncthreads();
    STAMP(3);
    const __amdgpu_buffer_rsrc_t rT = __builtin_amdgcn_make_buffer_rsrc((void*)TP, 0, (unsigned)N * 1024u, 0x00020000);
    const unsigned sw = ((((unsigned)(2 * w + (g >> 1))) ^ (unsigned)i16) * 16u) + (unsigned)(g & 1) * 8u;
#pragma unroll 1
    for (int t = 0; t < G_TILES; ++t) {
        char* ost = smem + GO_OST + (t & 1) * (16 * OP);
        char* orow = ost + i16 * OP + 32 * w + 8 * g;
        bf16x8 bh[4];
#pragma unroll
        for (int ks = 0; ks < 4; ++ks)
            bh[ks] = *reinterpret_cast<const bf16x8*>(smem + GO_H + (16 * t + i16) * HP + (32 * ks + 8 * g) * 2);
        *reinterpret_cast<u32x2*>(orow + 768) = *reinterpret_cast<const u32x2*>(smem + GO_S + (16 * t + i16) * 256 + sw);
        f32x4 c[3];
#pragma unroll
        for (int q = 0; q < 3; ++q) c[q] = f32x4{0.f, 0.f, 0.f, 0.f};
#pragma unroll
        for (int ks = 0; ks < 4; ++ks)
#pragma unroll
            for (int q = 0; q < 3; ++q) c[q] = __builtin_amdgcn_mfma_f32_16x16x32_bf16(a2[q][ks], bh[ks], c[q], 0, 0, 0);
#pragma unroll
        for (int q = 0; q < 3; ++q) {
            u32x2 o;
            o[0] = pack_bf16(c[q][0], c[q][1]);
            o[1] = pack_bf16(c[q][2], c[q][3]);
            *reinterpret_cast<u32x2*>(orow + q * 256) = o;
        }
        __syncthreads();
#pragma unroll
        for (int q = 0; q < 2; ++q) {
            const int r = 2 * w + q;
            const u32x4 o = *reinterpret_cast<const u32x4*>(ost + r * OP + lane * 16);
            __builtin_amdgcn_raw_buffer_store_b128(o, rT, (unsigned)(row0 + 16 * t + r) * 1024u + (unsigned)lane * 16u, 0, ST_AUX);
        }
    }
    STAMP(5);
}

constexpr int EPT = 8;
constexpr int CH = K1_THREADS * EPT;
constexpr int MAXNB = 128;
constexpr int EO_POS = 0;
constexpr int POS_LDS_MAX = 98304;
constexpr int EO_SRC = POS_LDS_MAX;
constexpr int EO_DST = EO_SRC + CH * 4;
constexpr int EO_RANK = EO_DST + CH * 4;
constexpr int EO_HIST = EO_RANK + CH * 2;
static_assert(EO_HIST + 8 * MAXNB * 4 <= K1_SMEM, "LDS");

struct Pos3 { float x, y, z; };
DI Pos3 unpack_pos(unsigned a, unsigned b, unsigned odd16) {
    const unsigned lo = __builtin_amdgcn_alignbit(b, a, odd16);
    const unsigned hi = b >> odd16;
    return Pos3{bf_lo(lo), bf_hi(lo), bf_lo(hi)};
}
DI Pos3 pos_lds(const char* lp, unsigned n) {
    const unsigned off = 6u * n;
    const unsigned* p = reinterpret_cast<const unsigned*>(lp + (off & ~3u));
    return unpack_pos(p[0], p[1], (off & 2u) << 3);
}
DI Pos3 pos_glb(const unsigned char* __restrict__ posb, unsigned n, unsigned lim) {
    const unsigned off = 6u * n;
    const unsigned a = off < lim ? off : lim;
    unsigned long long v;
    __builtin_memcpy(&v, posb + a, 8);
    v >>= (off - a) * 8u;
    return Pos3{bf_lo((unsigned)v), bf_hi((unsigned)v), bf_lo((unsigned)(v >> 32))};
}
DI float sin_rev(float r) { return __builtin_amdgcn_sinf(r - __builtin_rintf(r)); }
DI float cos_rev(float r) { return __builtin_amdgcn_cosf(r - __builtin_rintf(r)); }

template <bool POS_LDS>
DI void edge_block(int chunk, int nchunks, const int* __restrict__ src, const int* __restrict__ dst,
                   const unsigned char* __restrict__ posb, const unsigned short* __restrict__ freqs,
                   Rec* __restrict__ rec, unsigned* __restrict__ tab, int N, int E,
                   int BN, int NB, unsigned binmagic, char* smem) {
    const int tid = threadIdx.x;
    const int lane = tid & 63;
    const int w = __builtin_amdgcn_readfirstlane(tid >> 6);
    unsigned* it_src = reinterpret_cast<unsigned*>(smem + EO_SRC);
    unsigned* it_dst = reinterpret_cast<unsigned*>(smem + EO_DST);
    unsigned short* it_rank = reinterpret_cast<unsigned short*>(smem + EO_RANK);
    unsigned* hist = reinterpret_cast<unsigned*>(smem + EO_HIST);
    const unsigned lim = 6u * (unsigned)N - 8u;
    STAMP(0);
    const int wbase = chunk * CH + w * (EPT * 64);
    int es[EPT], ed[EPT];
#pragma unroll
    for (int j = 0; j < EPT / 4; ++j) {
        const int e = wbase + (j * 64 + lane) * 4;
        if (e + 3 < E) {
            const i32x4 s4 = *reinterpret_cast<const i32x4*>(src + e);
            const i32x4 d4 = *reinterpret_cast<const i32x4*>(dst + e);
#pragma unroll
            for (int q = 0; q < 4; ++q) { es[4 * j + q] = s4[q]; ed[4 * j + q] = d4[q]; }
        } else {
#pragma unroll
            for (int q = 0; q < 4; ++q) {
                es[4 * j + q] = (e + q < E) ? src[e + q] : -1;
                ed[4 * j + q] = (e + q < E) ? dst[e + q] : 0;
            }
        }
    }
    if (POS_LDS) {
        const unsigned bytes = 6u * (unsigned)N;
        const __amdgpu_buffer_rsrc_t rP = __builtin_amdgcn_make_buffer_rsrc((void*)posb, 0, bytes & ~3u, 0x00020000);
        const unsigned sm = lds_addr(smem);
        for (unsigned o = (unsigned)w * 1024u; o < bytes; o += 8192u) dma16(rP, o + (unsigned)lane * 16u, sm + EO_POS + o);
    }
    hist[tid] = 0; hist[tid + K1_THREADS] = 0;
    if (POS_LDS) {
        asm volatile("s_waitcnt vmcnt(0)" ::: "memory");
        __syncthreads();
        if ((N & 1) && tid == 0)
            *reinterpret_cast<unsigned*>(smem + EO_POS + ((6u * (unsigned)N) & ~3u)) =
                (unsigned)*reinterpret_cast<const unsigned short*>(posb + 6u * (unsigned)N - 2u);
        if (N & 1) __syncthreads();
    } else {
        __syncthreads();
    }
    STAMP(1);
    int cnt = 0;
#pragma unroll
    for (int j = 0; j < EPT; ++j) {
        const unsigned s = (unsigned)(es[j] < 0 ? 0 : es[j]), d = (unsigned)ed[j];
        const Pos3 a = POS_LDS ? pos_lds(smem + EO_POS, s) : pos_glb(posb, s, lim);
        const Pos3 b = POS_LDS ? pos_lds(smem + EO_POS, d) : pos_glb(posb, d, lim);
        const float rx = a.x - b.x, ry = a.y - b.y, rz = a.z - b.z;
        const float d2 = rx * rx + ry * ry + rz * rz + 1e-8f;
        const bool act = (es[j] >= 0) && !(d2 >= 25.0f && d2 < __builtin_inff());
        const unsigned long long m = __ballot(act);
        if (act) {
            const int p = w * (EPT * 64) + cnt + (int)mbcnt64(m);
            it_src[p] = s;
            it_dst[p] = d;
        }
        cnt += __popcll(m);
    }
    STAMP(2);
    unsigned* myhist = hist + w * MAXNB;
#pragma unroll 1
    for (int b = lane; b < cnt; b += 64) {
        const int p = w * (EPT * 64) + b;
        it_rank[p] = (unsigned short)atomicAdd(&myhist[__umulhi(it_dst[p], binmagic)], 1u);
    }
    __syncthreads();
    if (w == 0) {
        u32x2 h[8];
        unsigned t0 = 0, t1 = 0;
#pragma unroll
        for (int ww = 0; ww < 8; ++ww) {
            h[ww] = *reinterpret_cast<const u32x2*>(hist + ww * MAXNB + 2 * lane);
            t0 += h[ww][0]; t1 += h[ww][1];
        }
        unsigned r0 = wave_incl_scan(t0 + t1, lane) - t0 - t1, r1 = r0 + t0;
        const int ncp = (nchunks + 3) & ~3;
        if (2 * lane < NB) tab[(size_t)(2 * lane) * ncp + chunk] = r0 | (t0 << 16);
        if (2 * lane + 1 < NB) tab[(size_t)(2 * lane + 1) * ncp + chunk] = r1 | (t1 << 16);
#pragma unroll
        for (int ww = 0; ww < 8; ++ww) {
            *reinterpret_cast<u32x2*>(hist + ww * MAXNB + 2 * lane) = u32x2{r0, r1};
            r0 += h[ww][0]; r1 += h[ww][1];
        }
    }
    __syncthreads();
    STAMP(3);
    float fr[4];
#pragma unroll
    for (int k = 0; k < 4; ++k) fr[k] = bf_lo((unsigned)freqs[k]) * 0.15915494309189535f;
#pragma unroll 1
    for (int b = lane; b < cnt; b += 64) {
        const int p = w * (EPT * 64) + b;
        const unsigned s = it_src[p], dd = it_dst[p];
        const Pos3 a = POS_LDS ? pos_lds(smem + EO_POS, s) : pos_glb(posb, s, lim);
        const Pos3 c = POS_LDS ? pos_lds(smem + EO_POS, dd) : pos_glb(posb, dd, lim);
        const float rx = a.x - c.x, ry = a.y - c.y, rz = a.z - c.z;
        const unsigned bin = __umulhi(dd, binmagic);
        const unsigned dl = dd - bin * (unsigned)BN;
        const unsigned pos = myhist[bin] + (unsigned)it_rank[p];
        const float d2 = rx * rx + ry * ry + rz * rz + 1e-8f;
        const float inv = __builtin_amdgcn_rsqf(d2);
        const float d = d2 * inv;
        float fc = 0.5f * (cos_rev(d * 0.1f) + 1.0f);
        fc = (d < 5.0f) ? fc : 0.0f * d;
        const float sc = inv * fc;
        u32x4 r0, r1;
        r0[0] = s | (dl << 22);
        r0[1] = __float_as_uint(rx * inv);
        r0[2] = __float_as_uint(ry * inv);
        r0[3] = __float_as_uint(rz * inv);
        r1[0] = __float_as_uint(sin_rev(d * fr[0]) * sc);
        r1[1] = __float_as_uint(sin_rev(d * fr[1]) * sc);
        r1[2] = __float_as_uint(sin_rev(d * fr[2]) * sc);
        r1[3] = __float_as_uint(sin_rev(d * fr[3]) * sc);
        u32x4* o = reinterpret_cast<u32x4*>(rec + (size_t)chunk * CH + pos);
        o[0] = r0;
        o[1] = r1;
    }
    STAMP(5);
}

__global__ __launch_bounds__(K1_THREADS) void painn_k1(const unsigned short* __restrict__ sv,
                                                       const unsigned char* __restrict__ posb,
                                                       const int* __restrict__ src, const int* __restrict__ dst,
                                                       const unsigned short* __restrict__ freqs,
                                                       const unsigned short* __restrict__ w1,
                                                       const unsigned short* __restrict__ w2,
                                                       unsigned char* __restrict__ TP, Rec* __restrict__ rec,
                                                       unsigned* __restrict__ tab, int N, int E, int BN, int NB,
                                                       int nchunks, int ngemm, unsigned binmagic) {
    extern __shared__ __attribute__((aligned(16))) char smem[];
    const int b = blockIdx.x;
    STAMPX(0);
    if (b < nchunks) {
        if (6 * N + 16 <= POS_LDS_MAX)
            edge_block<true>(b, nchunks, src, dst, posb, freqs, rec, tab, N, E, BN, NB, binmagic, smem);
        else
            edge_block<false>(b, nchunks, src, dst, posb, freqs, rec, tab, N, E, BN, NB, binmagic, smem);
    } else {
        gemm_block(b - nchunks, sv, w1, w2, TP, N, smem);
    }
}

constexpr int K2_NW = 16;
constexpr int K2_THREADS = K2_NW * 64;
constexpr int K2_BN = 100;
constexpr int K2_NG = K2_NW * 2;
constexpr int K2_CAP = 2048;
constexpr int K2_ICAP = K2_CAP + K2_BN + K2_NG + 20;
constexpr int K2_MAXCH = 256;
#define RING_R 2
constexpr int K2_R = RING_R;
constexpr int K2_SLOT = 1536;
constexpr int O_RING = 0;
constexpr int O_ITEMS = K2_NW * K2_R * K2_SLOT;
constexpr int O_PERM = O_ITEMS + (K2_CAP + 1) * 32;
constexpr int O_SKEY = O_PERM + K2_ICAP * 2;
constexpr int O_NOFF = O_SKEY + K2_ICAP * 4;
constexpr int O_HEAD = O_NOFF + 128 * 4;
constexpr int O_WSUM = O_HEAD + K2_NG * 4;
constexpr int K2_SMEM = O_WSUM + 64;
static_assert(K2_SMEM <= 163840, "LDS");
static_assert(K2_ICAP % 8 == 0, "align");
static_assert(K2_CAP <= 2 * K2_THREADS, "rounds");
constexpr unsigned SRC_MASK = 0x3fffffu;
constexpr unsigned F_FIN = 0x80000000u, F_SPLIT = 0x40000000u;
#define OUT_AUX 2

DI void masked_zero8(float* d, unsigned long long mask) {
    unsigned long long sv;
    asm volatile("s_and_saveexec_b64 %8, %9\n\ts_cbranch_execz .Lmy%=\n\t"
                 "v_mov_b32 %0, 0\n\tv_mov_b32 %1, 0\n\tv_mov_b32 %2, 0\n\tv_mov_b32 %3, 0\n\t"
                 "v_mov_b32 %4, 0\n\tv_mov_b32 %5, 0\n\tv_mov_b32 %6, 0\n\tv_mov_b32 %7, 0\n"
                 ".Lmy%=:\n\ts_mov_b64 exec, %8"
                 : "+v"(d[0]), "+v"(d[1]), "+v"(d[2]), "+v"(d[3]), "+v"(d[4]), "+v"(d[5]), "+v"(d[6]), "+v"(d[7]), "=&s"(sv)
                 : "s"(mask) : "scc");
}

DI void k2_generic(const unsigned char* __restrict__ svb, const unsigned char* __restrict__ TP,
                   const unsigned short* __restrict__ wrbf, const Rec* __restrict__ rec, const unsigned* __restrict__ tab,
                   unsigned char* __restrict__ outb, int bin, int hf, int node0, int nn, int nchunks) {
    const int lane = threadIdx.x & 63;
    const int w = __builtin_amdgcn_readfirstlane(threadIdx.x >> 6);
    const int gC = lane >> 5, l32 = lane & 31;
    const int ncp = (nchunks + 3) & ~3;
    const size_t co = (size_t)(hf * 128 + l32 * 4);
    float wr[3][4][2];
#pragma unroll
    for (int sg = 0; sg < 3; ++sg)
#pragma unroll
        for (int k = 0; k < 4; ++k) {
            const unsigned u = *reinterpret_cast<const unsigned*>(wrbf + k * D3 + sg * D + 64 * hf + 2 * l32);
            wr[sg][k][0] = bf_lo(u); wr[sg][k][1] = bf_hi(u);
        }
#pragma unroll 1
    for (int j = w; j < nn; j += K2_NW) {
        float acc[8];
#pragma unroll
        for (int r = 0; r < 8; ++r) acc[r] = 0.f;
#pragma unroll 1
        for (int c = 0; c < nchunks; ++c) {
            const unsigned ent = tab[(size_t)bin * ncp + c];
            const int cnt = (int)(ent >> 16);
            const Rec* rp = rec + (size_t)c * CH + (ent & 0xffffu);
#pragma unroll 1
            for (int b = 0; b < cnt; b += 64) {
                const bool in = b + lane < cnt;
                const unsigned key = in ? rp[b + lane].key : 0u;
                unsigned long long m = __ballot(in && (key >> 22) == (unsigned)j);
#pragma unroll 1
                while (m) {
                    const int i0 = __builtin_ctzll(m);
                    m &= m - 1;
                    const int i1 = m ? __builtin_ctzll(m) : -1;
                    m &= m - 1;
                    const int im = gC ? i1 : i0;
                    if (im >= 0) {
                        const u32x4 rr = reinterpret_cast<const u32x4*>(rp + b + im)[0], kk = reinterpret_cast<const u32x4*>(rp + b + im)[1];
                        const size_t n = rr[0] & SRC_MASK;
                        const unsigned* pp = reinterpret_cast<const unsigned*>(TP + n * 1024 + co);
                        const unsigned* vp = reinterpret_cast<const unsigned*>(svb + n * 1024 + co);
                        const unsigned ps = pp[0], pv = pp[64], pr = pp[128], vx = vp[64], vy = vp[128], vz = vp[192];
#pragma unroll
                        for (int q = 0; q < 2; ++q) {
                            float wf[3];
#pragma unroll
                            for (int sg = 0; sg < 3; ++sg)
                                wf[sg] = __uint_as_float(kk[0]) * wr[sg][0][q] + __uint_as_float(kk[1]) * wr[sg][1][q] +
                                         __uint_as_float(kk[2]) * wr[sg][2][q] + __uint_as_float(kk[3]) * wr[sg][3][q];
                            const float mv = (q ? bf_hi(pv) : bf_lo(pv)) * wf[1], mr = (q ? bf_hi(pr) : bf_lo(pr)) * wf[2];
                            acc[q] += (q ? bf_hi(ps) : bf_lo(ps)) * wf[0];
                            acc[2 + q] += mv * (q ? bf_hi(vx) : bf_lo(vx)) + mr * __uint_as_float(rr[1]);
                            acc[4 + q] += mv * (q ? bf_hi(vy) : bf_lo(vy)) + mr * __uint_as_float(rr[2]);
                            acc[6 + q] += mv * (q ? bf_hi(vz) : bf_lo(vz)) + mr * __uint_as_float(rr[3]);
                        }
                    }
                }
            }
        }
#pragma unroll
        for (int r = 0; r < 8; ++r) acc[r] += __shfl_xor(acc[r], 32, 64);
        if (gC == 0) {
            const size_t n = (size_t)(node0 + j);
            const unsigned* bp = reinterpret_cast<const unsigned*>(svb + n * 1024 + co);
            unsigned* op = reinterpret_cast<unsigned*>(outb + n * 1024 + co);
#pragma unroll
            for (int r = 0; r < 4; ++r) {
                const unsigned b = bp[64 * r];
                op[64 * r] = pack_bf16(bf_lo(b) + acc[2 * r], bf_hi(b) + acc[2 * r + 1]);
            }
        }
    }
}

__global__ __launch_bounds__(K2_THREADS) void painn_k2(const unsigned char* __restrict__ svb,
                                                       const unsigned char* __restrict__ TP,
                                                       const unsigned short* __restrict__ wrbf,
                                                       const Rec* __restrict__ rec,
                                                       const unsigned* __restrict__ tab,
                                                       unsigned char* __restrict__ outb, int N, int BN, int NB, int nchunks) {
    extern __shared__ __attribute__((aligned(16))) char smem[];
    const int tid = threadIdx.x;
    const int lane = tid & 63;
    const int w = __builtin_amdgcn_readfirstlane(tid >> 6);
    const int hf = blockIdx.x & 1;
    const int bin = blockIdx.x >> 1;
    STAMP2(0); STAMPX(2048);
    const int node0 = bin * BN;
    const int nn = min(BN, N - node0);
    unsigned* hist = reinterpret_cast<unsigned*>(smem + O_RING);
    u32x4* items = reinterpret_cast<u32x4*>(smem + O_ITEMS);
    unsigned short* perm = reinterpret_cast<unsigned short*>(smem + O_PERM);
    unsigned* skey = reinterpret_cast<unsigned*>(smem + O_SKEY);
    unsigned* noff = reinterpret_cast<unsigned*>(smem + O_NOFF);
    unsigned* headinfo = reinterpret_cast<unsigned*>(smem + O_HEAD);
    unsigned* wsum = reinterpret_cast<unsigned*>(smem + O_WSUM);

    const __amdgpu_buffer_rsrc_t rTP = __builtin_amdgcn_make_buffer_rsrc((void*)TP, 0, (unsigned)N * 1024u, 0x00020000);
    const __amdgpu_buffer_rsrc_t rSV = __builtin_amdgcn_make_buffer_rsrc((void*)svb, 0, (unsigned)N * 1024u, 0x00020000);
    const __amdgpu_buffer_rsrc_t rOut = __builtin_amdgcn_make_buffer_rsrc((void*)outb, 0, (unsigned)N * 1024u, 0x00020000);
    const __amdgpu_buffer_rsrc_t rRec =
        __builtin_amdgcn_make_buffer_rsrc((void*)rec, 0, (unsigned)nchunks * CH * 32u, 0x00020000);

    const int ncp = (nchunks + 3) & ~3;
    u32x4 e = {0u, 0u, 0u, 0u};
    if (4 * lane < nchunks) e = *reinterpret_cast<const u32x4*>(tab + (size_t)bin * ncp + 4 * lane);
#pragma unroll
    for (int q = 1; q < 4; ++q) e[q] = 4 * lane + q < nchunks ? e[q] : 0u;
    unsigned wpk[3][4];
#pragma unroll
    for (int sg = 0; sg < 3; ++sg)
#pragma unroll
        for (int k = 0; k < 4; ++k)
            wpk[sg][k] = *reinterpret_cast<const unsigned*>(wrbf + k * D3 + sg * D + 64 * hf + 2 * (lane & 31));
    if (tid < 16 * K2_BN / 4) reinterpret_cast<u32x4*>(hist)[tid] = u32x4{0u, 0u, 0u, 0u};
    if (tid < 2) items[2 * K2_CAP + tid] = u32x4{0u, 0u, 0u, 0u};
    if (tid < K2_NG) headinfo[tid] = 0xffffffffu;
    STAMP2(1);
    int T;
    {
        const unsigned c0 = e[0] >> 16, c1 = e[1] >> 16, c2 = e[2] >> 16, c3 = e[3] >> 16;
        const unsigned incl = wave_incl_scan(c0 + c1 + c2 + c3, lane);
        T = __builtin_amdgcn_readlane((int)incl, 63);
        const unsigned o3 = incl - c3, o2 = o3 - c2, o1 = o2 - c1, o0 = o1 - c0;
        const unsigned me = (w & 3) == 0 ? e[0] : (w & 3) == 1 ? e[1] : (w & 3) == 2 ? e[2] : e[3];
        const unsigned mo = (w & 3) == 0 ? o0 : (w & 3) == 1 ? o1 : (w & 3) == 2 ? o2 : o3;
        const unsigned ib = lds_addr(items);
        if (T <= K2_CAP && nchunks <= K2_MAXCH && BN <= K2_BN) {
#pragma unroll 1
            for (int l = w >> 2; 4 * l + (w & 3) < nchunks; l += 4) {
                const unsigned ee = (unsigned)__builtin_amdgcn_readlane((int)me, l);
                const unsigned o = (unsigned)__builtin_amdgcn_readlane((int)mo, l);
                const unsigned cnt2 = 2u * (ee >> 16), gb = ((unsigned)(4 * l + (w & 3)) * CH + (ee & 0xffffu)) * 32u;
#pragma unroll 1
                for (unsigned i = 0; i < cnt2; i += 64u)
                    if (i + (unsigned)lane < cnt2) dma16(rRec, gb + (i + (unsigned)lane) * 16u, ib + o * 32u + i * 16u);
            }
        }
    }
    if (T > K2_CAP || nchunks > K2_MAXCH || BN > K2_BN) {
        asm volatile("s_waitcnt vmcnt(0)" ::: "memory");
        k2_generic(svb, TP, wrbf, rec, tab, outb, bin, hf, node0, nn, nchunks);
        return;
    }
    const int Tc = T;
    const int M = Tc + nn;
    const int L = (M + K2_NG - 1) / K2_NG;
    asm volatile("s_waitcnt vmcnt(0)" ::: "memory");
    __syncthreads();
    STAMP2(2);
    unsigned* myhist = hist + w * K2_BN;
    unsigned rdk[2] = {0u, 0u}, rkey[2] = {0u, 0u};
#pragma unroll
    for (int r = 0; r < 2; ++r)
        if (K2_THREADS * r + tid < Tc) {
            rkey[r] = items[2 * (K2_THREADS * r + tid)][0];
            const unsigned dl = rkey[r] >> 22;
            rdk[r] = dl | (atomicAdd(&myhist[dl], 1u) << 16);
        }
    __syncthreads();
    STAMP2(4);
    if (w < 2) {
        unsigned ntot = 0;
        if (tid < K2_BN) {
            unsigned h[16];
#pragma unroll
            for (int ww = 0; ww < 16; ++ww) h[ww] = hist[ww * K2_BN + tid];
#pragma unroll
            for (int ww = 0; ww < 16; ++ww) { hist[ww * K2_BN + tid] = ntot; ntot += h[ww]; }
        }
        const unsigned len = tid < nn ? ntot + 1u : 0u;
        const unsigned nincl = wave_incl_scan(len, lane);
        if (lane == 63) wsum[w] = nincl;
        noff[tid] = (nincl - len) | (ntot << 16);
    }
    __syncthreads();
    STAMP2(5);
    const unsigned w0tot = wsum[0];
    const unsigned Mdiv = 0xffffffffu / (unsigned)L + 1u;
#pragma unroll
    for (int r = 0; r < 2; ++r)
        if (K2_THREADS * r + tid < Tc) {
            const unsigned dl = rdk[r] & 0xffffu;
            const unsigned p = (noff[dl] & 0xffffu) + (dl >= 64u ? w0tot : 0u) + myhist[dl] + (rdk[r] >> 16);
            perm[p] = (unsigned short)(K2_THREADS * r + tid);
            skey[p] = rkey[r] & SRC_MASK;
        }
    if (tid < nn) {
        const unsigned no = noff[tid];
        const unsigned p0 = (no & 0xffffu) + (tid >= 64 ? w0tot : 0u), pf = p0 + (no >> 16);
        const unsigned g0 = L == 1 ? p0 : __umulhi(p0, Mdiv), gf = L == 1 ? pf : __umulhi(pf, Mdiv);
        perm[pf] = (unsigned short)K2_CAP;
        skey[pf] = (unsigned)(node0 + tid) | F_FIN | (g0 < gf ? F_SPLIT : 0u);
        if (g0 < gf) headinfo[gf] = g0;
    }
    if (tid >= 512 && M + (tid - 512) < K2_NG * L + 16) {
        perm[M + tid - 512] = (unsigned short)K2_CAP;
        skey[M + tid - 512] = 0u;
    }
    float wr[3][4][2];
#pragma unroll
    for (int sg = 0; sg < 3; ++sg)
#pragma unroll
        for (int k = 0; k < 4; ++k) { wr[sg][k][0] = bf_lo(wpk[sg][k]); wr[sg][k][1] = bf_hi(wpk[sg][k]); }
    __syncthreads();
    STAMP2(9);

    float acc[8], accH[8];
#pragma unroll
    for (int r = 0; r < 8; ++r) { acc[r] = 0.f; accH[r] = 0.f; }
    unsigned hnode = 0xffffffffu;
    const int gC = lane >> 5, l32 = lane & 31;
    const int gD = lane >= 24 ? 1 : 0, lD = lane - 24 * gD;
    const unsigned short* pp = perm + (2 * w + gC) * L;
    const unsigned* pkC = skey + (2 * w + gC) * L;
    const unsigned* pkD = skey + (2 * w + gD) * L;
    const unsigned offP = (unsigned)((lD >> 3) * 256 + hf * 128 + (lD & 7) * 16);
    const unsigned offS = lD < 8 ? (unsigned)(768 + hf * 128 + lD * 16) : 0x80000000u;
    const unsigned offV = offP + 256u;
    const unsigned ringb = lds_addr(smem + O_RING + w * (K2_R * K2_SLOT));
    const char* ringp = smem + O_RING + w * (K2_R * K2_SLOT) + gC * 384 + l32 * 4;

    auto issue = [&](unsigned key, int slot) LAM {
        const unsigned ro = (key & SRC_MASK) * 1024u;
        if (lane < 48) {
            dma16g(rTP, ro + ((key & F_FIN) ? offS : offP), ringb + (unsigned)slot * K2_SLOT);
            dma16g(rSV, ro + offV, ringb + (unsigned)slot * K2_SLOT + 768u);
        }
    };
    auto compute = [&](int slot, unsigned q, unsigned key) LAM {
        const u32x4 rr = items[2 * q], kk = items[2 * q + 1];
        const unsigned* dp = reinterpret_cast<const unsigned*>(ringp + slot * K2_SLOT);
        asm volatile("s_waitcnt vmcnt(%0)" :: "n"(2 * (K2_R - 1)) : "memory");
        const unsigned ps = dp[0], pv = dp[32], pr = dp[64], vx = dp[192], vy = dp[224], vz = dp[256];
        const float hx = __uint_as_float(rr[1]), hy = __uint_as_float(rr[2]), hz = __uint_as_float(rr[3]);
        const float c0 = __uint_as_float(kk[0]), c1 = __uint_as_float(kk[1]), c2 = __uint_as_float(kk[2]), c3 = __uint_as_float(kk[3]);
        float u[8];
#pragma unroll
        for (int j = 0; j < 2; ++j) {
            u[j] = j ? bf_hi(ps) : bf_lo(ps);
            u[2 + j] = j ? bf_hi(vx) : bf_lo(vx);
            u[4 + j] = j ? bf_hi(vy) : bf_lo(vy);
            u[6 + j] = j ? bf_hi(vz) : bf_lo(vz);
            const float fv = j ? bf_hi(pv) : bf_lo(pv), fr = j ? bf_hi(pr) : bf_lo(pr);
            float wfs, wfv, wfr;
            asm("v_mul_f32 %0, %3, %7\n\tv_mul_f32 %1, %3, %11\n\tv_mul_f32 %2, %3, %15\n\t"
                "v_fmac_f32 %0, %4, %8\n\tv_fmac_f32 %1, %4, %12\n\tv_fmac_f32 %2, %4, %16\n\t"
                "v_fmac_f32 %0, %5, %9\n\tv_fmac_f32 %1, %5, %13\n\tv_fmac_f32 %2, %5, %17\n\t"
                "v_fmac_f32 %0, %6, %10\n\tv_fmac_f32 %1, %6, %14\n\tv_fmac_f32 %2, %6, %18\n\t"
                "v_mul_f32 %1, %19, %1\n\tv_mul_f32 %2, %20, %2"
                : "=&v"(wfs), "=&v"(wfv), "=&v"(wfr)
                : "v"(c0), "v"(c1), "v"(c2), "v"(c3), "v"(wr[0][0][j]), "v"(wr[0][1][j]), "v"(wr[0][2][j]), "v"(wr[0][3][j]),
                  "v"(wr[1][0][j]), "v"(wr[1][1][j]), "v"(wr[1][2][j]), "v"(wr[1][3][j]),
                  "v"(wr[2][0][j]), "v"(wr[2][1][j]), "v"(wr[2][2][j]), "v"(wr[2][3][j]), "v"(fv), "v"(fr));
            asm("v_fmac_f32 %0, %4, %5\n\tv_fmac_f32 %1, %6, %8\n\tv_fmac_f32 %2, %6, %9\n\tv_fmac_f32 %3, %6, %10\n\t"
                "v_fmac_f32 %1, %7, %11\n\tv_fmac_f32 %2, %7, %12\n\tv_fmac_f32 %3, %7, %13"
                : "+v"(acc[j]), "+v"(acc[2 + j]), "+v"(acc[4 + j]), "+v"(acc[6 + j])
                : "v"(u[j]), "v"(wfs), "v"(wfv), "v"(wfr), "v"(u[2 + j]), "v"(u[4 + j]), "v"(u[6 + j]), "v"(hx), "v"(hy), "v"(hz));
        }
        const bool fin = (int)key < 0;
        if (__ballot(fin) != 0ull) {
            float t[8];
#pragma unroll
            for (int r = 0; r < 8; ++r) t[r] = acc[r] + u[r];
            const bool split = (key & F_SPLIT) != 0u;
            const unsigned oo = (fin && !split) ? (key & SRC_MASK) * 1024u + (unsigned)(hf * 128 + l32 * 4) : 0x80000000u;
#pragma unroll
            for (int r = 0; r < 4; ++r)
                __builtin_amdgcn_raw_buffer_store_b32(pack_bf16(t[2 * r], t[2 * r + 1]), rOut, oo + (unsigned)r * 256u, 0, OUT_AUX);
            masked_copy8(accH, t, __ballot(fin && split));
            hnode = (fin && split) ? (key & SRC_MASK) : hnode;
            masked_zero8(acc, __ballot(fin));
        }
    };

    {
#pragma unroll
        for (int k = 0; k < K2_R - 1; ++k) issue(pkD[k], k);
        unsigned kD = pkD[K2_R - 1], q = pp[0], kC = pkC[0];
        int slot = 0, fslot = K2_R - 1;
#pragma unroll 1
        for (int it = 0; it < L; ++it) {
            const unsigned kDn = pkD[it + K2_R], qn = pp[it + 1], kCn = pkC[it + 1];
            issue(kD, fslot);
            compute(slot, q, kC);
            fslot = slot;
            slot = slot + 1 == K2_R ? 0 : slot + 1;
            kD = kDn; q = qn; kC = kCn;
        }
        asm volatile("s_waitcnt vmcnt(0)" ::: "memory");
    }
    STAMP2(10);
    __syncthreads();
    STAMP2(11);
    {
        float* tl = reinterpret_cast<float*>(smem + O_RING);
        const int G = 2 * w + gC;
#pragma unroll
        for (int r = 0; r < 8; ++r) tl[(G * 8 + r) * 32 + l32] = acc[r];
        __syncthreads();
        const unsigned g0 = headinfo[G];
        if (g0 != 0xffffffffu) {
            float t[8];
#pragma unroll
            for (int r = 0; r < 8; ++r) t[r] = tl[(g0 * 8 + r) * 32 + l32];
#pragma unroll 1
            for (int gg = (int)g0 + 1; gg < G; ++gg) {
#pragma unroll
                for (int r = 0; r < 8; ++r) t[r] += tl[(gg * 8 + r) * 32 + l32];
            }
            const unsigned oo = hnode * 1024u + (unsigned)(hf * 128 + l32 * 4);
#pragma unroll
            for (int r = 0; r < 4; ++r)
                __builtin_amdgcn_raw_buffer_store_b32(pack_bf16(t[2 * r] + accH[2 * r], t[2 * r + 1] + accH[2 * r + 1]), rOut,
                                                      oo + (unsigned)r * 256u, 0, OUT_AUX);
        }
    }
    STAMP2(13);
}

extern "C" void kernel_launch(void* const* d_in, const int* in_sizes, int n_in, void* d_out, int out_size,
                              void* d_ws, size_t ws_size, hipStream_t stream) {
    const unsigned short* sv = (const unsigned short*)d_in[0];
    const unsigned char* pos = (const unsigned char*)d_in[1];
    const int* src = (const int*)d_in[2];
    const int* dst = (const int*)d_in[3];
    const unsigned short* freqs = (const unsigned short*)d_in[4];
    const unsigned short* w1 = (const unsigned short*)d_in[5];
    const unsigned short* w2 = (const unsigned short*)d_in[6];
    const unsigned short* wrbf = (const unsigned short*)d_in[7];
    const int N = in_sizes[0] / (4 * D);
    const int E = in_sizes[2];

    const int BN = (N + MAXNB - 1) / MAXNB;
    const int NB = (N + BN - 1) / BN;
    const int nchunks = (E + CH - 1) / CH;
    const int ngemm = (N + G_ROWS - 1) / G_ROWS;
    const unsigned binmagic = (unsigned)(0xffffffffull / (unsigned)BN + 1ull);

    char* ws = (char*)d_ws;
    unsigned char* TP = (unsigned char*)ws;
    size_t off = ((size_t)N * 1024 + 255) & ~(size_t)255;
    Rec* rec = (Rec*)(ws + off);
    off += (size_t)nchunks * CH * sizeof(Rec);
    unsigned* tab = (unsigned*)(ws + off);

    (void)hipFuncSetAttribute((const void*)painn_k1, hipFuncAttributeMaxDynamicSharedMemorySize, K1_SMEM);
    painn_k1<<<ngemm + nchunks, K1_THREADS, K1_SMEM, stream>>>(sv, pos, src, dst, freqs, w1, w2, TP, rec, tab, N, E,
                                                                BN, NB, nchunks, ngemm, binmagic);
    (void)hipFuncSetAttribute((const void*)painn_k2, hipFuncAttributeMaxDynamicSharedMemorySize, K2_SMEM);
    painn_k2<<<NB * 2, K2_THREADS, K2_SMEM, stream>>>((const unsigned char*)sv, TP, wrbf, rec, tab, (unsigned char*)d_out, N, BN, NB,
                                                      nchunks);
}
```

```cpp
#include <hip/hip_runtime.h>
#include <hip/hip_bf16.h>
#include <stdint.h>

using bf16x8 = __attribute__((ext_vector_type(8))) short;
using f32x4 = __attribute__((ext_vector_type(4))) float;
using u32x4 = __attribute__((ext_vector_type(4))) unsigned;
using u32x2 = __attribute__((ext_vector_type(2))) unsigned;
using i32x4 = __attribute__((ext_vector_type(4))) int;
typedef __bf16 bf16x2_t __attribute__((ext_vector_type(2)));
typedef float f32x2 __attribute__((ext_vector_type(2)));

#define DI __device__ __forceinline__
#define LAM __attribute__((always_inline))
DI void masked_copy8(float* d, const float* s, unsigned long long mask) {
    unsigned long long sv;
    asm volatile("s_and_saveexec_b64 %8, %17\n\ts_cbranch_execz .Lmc%=\n\t"
                 "v_mov_b32 %0, %9\n\tv_mov_b32 %1, %10\n\tv_mov_b32 %2, %11\n\tv_mov_b32 %3, %12\n\t"
                 "v_mov_b32 %4, %13\n\tv_mov_b32 %5, %14\n\tv_mov_b32 %6, %15\n\tv_mov_b32 %7, %16\n"
                 ".Lmc%=:\n\ts_mov_b64 exec, %8"
                 : "+v"(d[0]), "+v"(d[1]), "+v"(d[2]), "+v"(d[3]), "+v"(d[4]), "+v"(d[5]), "+v"(d[6]), "+v"(d[7]), "=&s"(sv)
                 : "v"(s[0]), "v"(s[1]), "v"(s[2]), "v"(s[3]), "v"(s[4]), "v"(s[5]), "v"(s[6]), "v"(s[7]), "s"(mask)
                 : "scc");
}
#define STAMP(i)
#define STAMP2(i)
#define STAMPX(i)

constexpr int D = 128;
constexpr int D3 = 384;

DI float bf_lo(unsigned u) { return __uint_as_float(u << 16); }
DI float bf_hi(unsigned u) { return __uint_as_float(u & 0xffff0000u); }
DI unsigned pack_bf16(float lo, float hi) {
    f32x2 v = {lo, hi};
    bf16x2_t b = __builtin_convertvector(v, bf16x2_t);
    return __builtin_bit_cast(unsigned, b);
}
DI float silu_f(float x) { return x * __builtin_amdgcn_rcpf(1.0f + __expf(-x)); }

struct Rec {
    unsigned key;
    float rh[3];
    float c[4];
};

typedef __attribute__((address_space(3))) void* lds_ptr_t;
typedef short s16x4 __attribute__((ext_vector_type(4)));
typedef __attribute__((address_space(3))) s16x4* lds_s16x4_ptr;

DI unsigned wave_incl_scan(unsigned v, int lane) {
    v += (unsigned)__builtin_amdgcn_update_dpp(0, (int)v, 0x111, 0xf, 0xf, false);
    v += (unsigned)__builtin_amdgcn_update_dpp(0, (int)v, 0x112, 0xf, 0xf, false);
    v += (unsigned)__builtin_amdgcn_update_dpp(0, (int)v, 0x114, 0xf, 0xf, false);
    v += (unsigned)__builtin_amdgcn_update_dpp(0, (int)v, 0x118, 0xf, 0xf, false);
    v += (unsigned)__builtin_amdgcn_update_dpp(0, (int)v, 0x142, 0xa, 0xf, false);
    v += (unsigned)__builtin_amdgcn_update_dpp(0, (int)v, 0x143, 0xc, 0xf, false);
    return v;
}
DI unsigned mbcnt64(unsigned long long m) {
    return __builtin_amdgcn_mbcnt_hi((unsigned)(m >> 32), __builtin_amdgcn_mbcnt_lo((unsigned)m, 0));
}
DI void dma16(const __amdgpu_buffer_rsrc_t rsrc, unsigned voff, unsigned lds_dst) {
    unsigned keep;
    asm volatile("s_mov_b32 %0, m0\n\ts_mov_b32 m0, %3\n\ts_nop 0\n\tbuffer_load_dwordx4 %1, %2, 0 offen lds\n\ts_mov_b32 m0, %0"
                 : "=&s"(keep) : "v"(voff), "s"(rsrc), "s"(lds_dst) : "memory");
}
#define GATHER_BITS "sc1"
DI void dma16g(const __amdgpu_buffer_rsrc_t rsrc, unsigned voff, unsigned lds_dst) {
    unsigned keep;
    asm volatile("s_mov_b32 %0, m0\n\ts_mov_b32 m0, %3\n\ts_nop 0\n\tbuffer_load_dwordx4 %1, %2, 0 offen " GATHER_BITS " lds\n\ts_mov_b32 m0, %0"
                 : "=&s"(keep) : "v"(voff), "s"(rsrc), "s"(lds_dst) : "memory");
}
DI unsigned lds_addr(const void* p) { return (unsigned)(size_t)(lds_ptr_t)p; }

constexpr int K1_THREADS = 512;
constexpr int G_TILES = 5;
constexpr int G_ROWS = 16 * G_TILES;
constexpr int HP = 272;
constexpr int GO_S = 131072;
constexpr int GO_H = 0;
constexpr int GO_OST = 32768;
constexpr int OP = 1040;
constexpr int K1_SMEM = 163840;
#define ST_AUX 16

DI unsigned wswz(unsigned row) { return ((row & 3u) << 2) | ((row >> 2) & 3u); }

DI void gemm_block(int blk, const unsigned short* __restrict__ sv, const unsigned short* __restrict__ w1,
                   const unsigned short* __restrict__ w2, unsigned char* __restrict__ TP, int N, char* smem) {
    const int tid = threadIdx.x;
    const int lane = tid & 63;
    const int w = __builtin_amdgcn_readfirstlane(tid >> 6);
    const int i16 = lane & 15, g = lane >> 4;
    const int row0 = blk * G_ROWS;
    const unsigned sm = lds_addr(smem);
    STAMP(0);
    const __amdgpu_buffer_rsrc_t rW1 = __builtin_amdgcn_make_buffer_rsrc((void*)w1, 0, D * D * 2, 0x00020000);
    const __amdgpu_buffer_rsrc_t rW2 = __builtin_amdgcn_make_buffer_rsrc((void*)w2, 0, D * D3 * 2, 0x00020000);
    const __amdgpu_buffer_rsrc_t rSV = __builtin_amdgcn_make_buffer_rsrc((void*)sv, 0, (unsigned)N * 1024u, 0x00020000);
    const unsigned rot = (unsigned)blk * 5u;

#pragma unroll
    for (int q = 0; q < 4; ++q) {
        const unsigned i = ((unsigned)(w + 8 * q) + rot) & 31u;
        const unsigned row = 4u * i + (unsigned)g;
        dma16(rW1, row * 256u + (((unsigned)i16 ^ wswz(row)) * 16u), sm + i * 1024u);
    }
#pragma unroll
    for (int q = 0; q < 3; ++q) {
        const unsigned i = (unsigned)(w + 8 * q);
        if (i < G_ROWS / 4) {
            const unsigned node = 4u * i + (unsigned)g;
            dma16(rSV, ((unsigned)row0 + node) * 1024u + (((unsigned)i16 ^ (node & 15u)) * 16u), sm + GO_S + i * 1024u);
        }
    }
#pragma unroll
    for (int q = 0; q < 12; ++q) {
        const unsigned i = ((unsigned)(w + 8 * (q & 3)) + rot) & 31u, seg = (unsigned)(q >> 2);
        const unsigned row = 4u * i + (unsigned)g;
        dma16(rW2, row * 768u + seg * 256u + (((unsigned)i16 ^ wswz(row)) * 16u), sm + 32768u * (seg + 1u) + i * 1024u);
    }
    asm volatile("s_waitcnt vmcnt(12)" ::: "memory");
    __syncthreads();
    STAMP(1);
    const unsigned tq = (unsigned)i16 >> 2, tp = (unsigned)lane & 3u;
    auto afrag = [&](int im, int ks) LAM {
        const unsigned row = 32u * ks + 8u * g + tq;
        const s16x4 lo = __builtin_amdgcn_ds_read_tr16_b64_v4i16((lds_s16x4_ptr)(smem + im * 32768 + 256u * row +
                            16u * ((2u * w + (tp >> 1)) ^ wswz(row)) + 8u * (tp & 1u)));
        const s16x4 hi = __builtin_amdgcn_ds_read_tr16_b64_v4i16((lds_s16x4_ptr)(smem + im * 32768 + 256u * (row + 4u) +
                            16u * ((2u * w + (tp >> 1)) ^ wswz(row + 4u)) + 8u * (tp & 1u)));
        return bf16x8{lo[0], lo[1], lo[2], lo[3], hi[0], hi[1], hi[2], hi[3]};
    };
    bf16x8 a1[4];
#pragma unroll
    for (int ks = 0; ks < 4; ++ks) a1[ks] = afrag(0, ks);
    __syncthreads();
#pragma unroll 1
    for (int t = 0; t < G_TILES; ++t) {
        const char* st = smem + GO_S + (16 * t + i16) * 256;
        f32x4 c = {0.f, 0.f, 0.f, 0.f};
#pragma unroll
        for (int ks = 0; ks < 4; ++ks) {
            const bf16x8 b = *reinterpret_cast<const bf16x8*>(st + (((unsigned)(4 * ks + g) ^ (unsigned)i16) * 16u));
            c = __builtin_amdgcn_mfma_f32_16x16x32_bf16(a1[ks], b, c, 0, 0, 0);
        }
        u32x2 o;
        o[0] = pack_bf16(silu_f(c[0]), silu_f(c[1]));
        o[1] = pack_bf16(silu_f(c[2]), silu_f(c[3]));
        *reinterpret_cast<u32x2*>(smem + GO_H + (16 * t + i16) * HP + (16 * w + 4 * g) * 2) = o;
    }
    STAMP(2);
    asm volatile("s_waitcnt vmcnt(0)" ::: "memory");
    __syncthreads();
    bf16x8 a2[3][4];
#pragma unroll
    for (int q = 0; q < 3; ++q)
#pragma unroll
        for (int ks = 0; ks < 4; ++ks) a2[q][ks] = afrag(q + 1, ks);
    __syncthreads();
    STAMP(3);
    const __amdgpu_buffer_rsrc_t rT = __builtin_amdgcn_make_buffer_rsrc((void*)TP, 0, (unsigned)N * 1024u, 0x00020000);
    const unsigned sw = ((((unsigned)(2 * w + (g >> 1))) ^ (unsigned)i16) * 16u) + (unsigned)(g & 1) * 8u;
#pragma unroll 1
    for (int t = 0; t < G_TILES; ++t) {
        char* ost = smem + GO_OST + (t & 1) * (16 * OP);
        char* orow = ost + i16 * OP + 32 * w + 8 * g;
        bf16x8 bh[4];
#pragma unroll
        for (int ks = 0; ks < 4; ++ks)
            bh[ks] = *reinterpret_cast<const bf16x8*>(smem + GO_H + (16 * t + i16) * HP + (32 * ks + 8 * g) * 2);
        *reinterpret_cast<u32x2*>(orow + 768) = *reinterpret_cast<const u32x2*>(smem + GO_S + (16 * t + i16) * 256 + sw);
        f32x4 c[3];
#pragma unroll
        for (int q = 0; q < 3; ++q) c[q] = f32x4{0.f, 0.f, 0.f, 0.f};
#pragma unroll
        for (int ks = 0; ks < 4; ++ks)
#pragma unroll
            for (int q = 0; q < 3; ++q) c[q] = __builtin_amdgcn_mfma_f32_16x16x32_bf16(a2[q][ks], bh[ks], c[q], 0, 0, 0);
#pragma unroll
        for (int q = 0; q < 3; ++q) {
            u32x2 o;
            o[0] = pack_bf16(c[q][0], c[q][1]);
            o[1] = pack_bf16(c[q][2], c[q][3]);
            *reinterpret_cast<u32x2*>(orow + q * 256) = o;
        }
        __syncthreads();
#pragma unroll
        for (int q = 0; q < 2; ++q) {
            const int r = 2 * w + q;
            const u32x4 o = *reinterpret_cast<const u32x4*>(ost + r * OP + lane * 16);
            __builtin_amdgcn_raw_buffer_store_b128(o, rT, (unsigned)(row0 + 16 * t + r) * 1024u + (unsigned)lane * 16u, 0, ST_AUX);
        }
    }
    STAMP(5);
}

constexpr int EPT = 8;
constexpr int CH = K1_THREADS * EPT;
#define NBINS 128
constexpr int MAXNB = NBINS;
constexpr int BPL = MAXNB / 64;
constexpr int EO_POS = 0;
constexpr int POS_LDS_MAX = 98304;
constexpr int EO_SRC = POS_LDS_MAX;
constexpr int EO_DST = EO_SRC + CH * 4;
constexpr int EO_RANK = EO_DST + CH * 4;
constexpr int EO_HIST = EO_RANK + CH * 2;
static_assert(EO_HIST + 8 * MAXNB * 4 <= K1_SMEM, "LDS");

struct Pos3 { float x, y, z; };
DI Pos3 unpack_pos(unsigned a, unsigned b, unsigned odd16) {
    const unsigned lo = __builtin_amdgcn_alignbit(b, a, odd16);
    const unsigned hi = b >> odd16;
    return Pos3{bf_lo(lo), bf_hi(lo), bf_lo(hi)};
}
DI Pos3 pos_lds(const char* lp, unsigned n) {
    const unsigned off = 6u * n;
    const unsigned* p = reinterpret_cast<const unsigned*>(lp + (off & ~3u));
    return unpack_pos(p[0], p[1], (off & 2u) << 3);
}
DI Pos3 pos_glb(const unsigned char* __restrict__ posb, unsigned n, unsigned lim) {
    const unsigned off = 6u * n;
    const unsigned a = off < lim ? off : lim;
    unsigned long long v;
    __builtin_memcpy(&v, posb + a, 8);
    v >>= (off - a) * 8u;
    return Pos3{bf_lo((unsigned)v), bf_hi((unsigned)v), bf_lo((unsigned)(v >> 32))};
}
DI float sin_rev(float r) { return __builtin_amdgcn_sinf(r - __builtin_rintf(r)); }
DI float cos_rev(float r) { return __builtin_amdgcn_cosf(r - __builtin_rintf(r)); }

template <bool POS_LDS>
DI void edge_block(int chunk, int nchunks, const int* __restrict__ src, const int* __restrict__ dst,
                   const unsigned char* __restrict__ posb, const unsigned short* __restrict__ freqs,
                   Rec* __restrict__ rec, unsigned* __restrict__ tab, int N, int E,
                   int BN, int NB, unsigned binmagic, char* smem) {
    const int tid = threadIdx.x;
    const int lane = tid & 63;
    const int w = __builtin_amdgcn_readfirstlane(tid >> 6);
    unsigned* it_src = reinterpret_cast<unsigned*>(smem + EO_SRC);
    unsigned* it_dst = reinterpret_cast<unsigned*>(smem + EO_DST);
    unsigned short* it_rank = reinterpret_cast<unsigned short*>(smem + EO_RANK);
    unsigned* hist = reinterpret_cast<unsigned*>(smem + EO_HIST);
    const unsigned lim = 6u * (unsigned)N - 8u;
    STAMP(0);
    const int wbase = chunk * CH + w * (EPT * 64);
    int es[EPT], ed[EPT];
#pragma unroll
    for (int j = 0; j < EPT / 4; ++j) {
        const int e = wbase + (j * 64 + lane) * 4;
        if (e + 3 < E) {
            const i32x4 s4 = *reinterpret_cast<const i32x4*>(src + e);
            const i32x4 d4 = *reinterpret_cast<const i32x4*>(dst + e);
#pragma unroll
            for (int q = 0; q < 4; ++q) { es[4 * j + q] = s4[q]; ed[4 * j + q] = d4[q]; }
        } else {
#pragma unroll
            for (int q = 0; q < 4; ++q) {
                es[4 * j + q] = (e + q < E) ? src[e + q] : -1;
                ed[4 * j + q] = (e + q < E) ? dst[e + q] : 0;
            }
        }
    }
    if (POS_LDS) {
        const unsigned bytes = 6u * (unsigned)N;
        const __amdgpu_buffer_rsrc_t rP = __builtin_amdgcn_make_buffer_rsrc((void*)posb, 0, bytes & ~3u, 0x00020000);
        const unsigned sm = lds_addr(smem);
        for (unsigned o = (unsigned)w * 1024u; o < bytes; o += 8192u) dma16(rP, o + (unsigned)lane * 16u, sm + EO_POS + o);
    }
#pragma unroll
    for (int i = 0; i < 8 * MAXNB / K1_THREADS; ++i) hist[tid + i * K1_THREADS] = 0;
    if (POS_LDS) {
        asm volatile("s_waitcnt vmcnt(0)" ::: "memory");
        __syncthreads();
        if ((N & 1) && tid == 0)
            *reinterpret_cast<unsigned*>(smem + EO_POS + ((6u * (unsigned)N) & ~3u)) =
                (unsigned)*reinterpret_cast<const unsigned short*>(posb + 6u * (unsigned)N - 2u);
        if (N & 1) __syncthreads();
    } else {
        __syncthreads();
    }
    STAMP(1);
    int cnt = 0;
#pragma unroll
    for (int j = 0; j < EPT; ++j) {
        const unsigned s = (unsigned)(es[j] < 0 ? 0 : es[j]), d = (unsigned)ed[j];
        const Pos3 a = POS_LDS ? pos_lds(smem + EO_POS, s) : pos_glb(posb, s, lim);
        const Pos3 b = POS_LDS ? pos_lds(smem + EO_POS, d) : pos_glb(posb, d, lim);
        const float rx = a.x - b.x, ry = a.y - b.y, rz = a.z - b.z;
        const float d2 = rx * rx + ry * ry + rz * rz + 1e-8f;
        const bool act = (es[j] >= 0) && !(d2 >= 25.0f && d2 < __builtin_inff());
        const unsigned long long m = __ballot(act);
        if (act) {
            const int p = w * (EPT * 64) + cnt + (int)mbcnt64(m);
            it_src[p] = s;
            it_dst[p] = d;
        }
        cnt += __popcll(m);
    }
    STAMP(2);
    unsigned* myhist = hist + w * MAXNB;
#pragma unroll 1
    for (int b = lane; b < cnt; b += 64) {
        const int p = w * (EPT * 64) + b;
        it_rank[p] = (unsigned short)atomicAdd(&myhist[__umulhi(it_dst[p], binmagic)], 1u);
    }
    __syncthreads();
    if (w == 0) {
        unsigned h[8][BPL], t[BPL], tsum = 0;
#pragma unroll
        for (int q = 0; q < BPL; ++q) {
            t[q] = 0;
#pragma unroll
            for (int ww = 0; ww < 8; ++ww) { h[ww][q] = hist[ww * MAXNB + BPL * lane + q]; t[q] += h[ww][q]; }
            tsum += t[q];
        }
        unsigned run = wave_incl_scan(tsum, lane) - tsum;
        const int ncp = (nchunks + 3) & ~3;
#pragma unroll
        for (int q = 0; q < BPL; ++q) {
            if (BPL * lane + q < NB) tab[(size_t)(BPL * lane + q) * ncp + chunk] = run | (t[q] << 16);
            unsigned r = run;
#pragma unroll
            for (int ww = 0; ww < 8; ++ww) { hist[ww * MAXNB + BPL * lane + q] = r; r += h[ww][q]; }
            run += t[q];
        }
    }
    __syncthreads();
    STAMP(3);
    float fr[4];
#pragma unroll
    for (int k = 0; k < 4; ++k) fr[k] = bf_lo((unsigned)freqs[k]) * 0.15915494309189535f;
#pragma unroll 1
    for (int b = lane; b < cnt; b += 64) {
        const int p = w * (EPT * 64) + b;
        const unsigned s = it_src[p], dd = it_dst[p];
        const Pos3 a = POS_LDS ? pos_lds(smem + EO_POS, s) : pos_glb(posb, s, lim);
        const Pos3 c = POS_LDS ? pos_lds(smem + EO_POS, dd) : pos_glb(posb, dd, lim);
        const float rx = a.x - c.x, ry = a.y - c.y, rz = a.z - c.z;
        const unsigned bin = __umulhi(dd, binmagic);
        const unsigned dl = dd - bin * (unsigned)BN;
        const unsigned pos = myhist[bin] + (unsigned)it_rank[p];
        const float d2 = rx * rx + ry * ry + rz * rz + 1e-8f;
        const float inv = __builtin_amdgcn_rsqf(d2);
        const float d = d2 * inv;
        float fc = 0.5f * (cos_rev(d * 0.1f) + 1.0f);
        fc = (d < 5.0f) ? fc : 0.0f * d;
        const float sc = inv * fc;
        u32x4 r0, r1;
        r0[0] = s | (dl << 22);
        r0[1] = __float_as_uint(rx * inv);
        r0[2] = __float_as_uint(ry * inv);
        r0[3] = __float_as_uint(rz * inv);
        r1[0] = __float_as_uint(sin_rev(d * fr[0]) * sc);
        r1[1] = __float_as_uint(sin_rev(d * fr[1]) * sc);
        r1[2] = __float_as_uint(sin_rev(d * fr[2]) * sc);
        r1[3] = __float_as_uint(sin_rev(d * fr[3]) * sc);
        u32x4* o = reinterpret_cast<u32x4*>(rec + (size_t)chunk * CH + pos);
        o[0] = r0;
        o[1] = r1;
    }
    STAMP(5);
}

__global__ __launch_bounds__(K1_THREADS) void painn_k1(const unsigned short* __restrict__ sv,
                                                       const unsigned char* __restrict__ posb,
                                                       const int* __restrict__ src, const int* __restrict__ dst,
                                                       const unsigned short* __restrict__ freqs,
                                                       const unsigned short* __restrict__ w1,
                                                       const unsigned short* __restrict__ w2,
                                                       unsigned char* __restrict__ TP, Rec* __restrict__ rec,
                                                       unsigned* __restrict__ tab, int N, int E, int BN, int NB,
                                                       int nchunks, int ngemm, unsigned binmagic) {
    extern __shared__ __attribute__((aligned(16))) char smem[];
    const int b = blockIdx.x;
    STAMPX(0);
    if (b < nchunks) {
        if (6 * N + 16 <= POS_LDS_MAX)
            edge_block<true>(b, nchunks, src, dst, posb, freqs, rec, tab, N, E, BN, NB, binmagic, smem);
        else
            edge_block<false>(b, nchunks, src, dst, posb, freqs, rec, tab, N, E, BN, NB, binmagic, smem);
    } else {
        gemm_block(b - nchunks, sv, w1, w2, TP, N, smem);
    }
}

#define K2_WAVES 16
constexpr int K2_NW = K2_WAVES;
constexpr int K2_THREADS = K2_NW * 64;
constexpr int K2_BN = (12800 / MAXNB + 3) & ~3;
constexpr int K2_NG = K2_NW * 2;
#define K2CAP 2048
constexpr int K2_CAP = K2CAP;
constexpr int K2_ICAP = (K2_CAP + K2_BN + K2_NG + 16 + 7) & ~7;
constexpr int K2_MAXCH = 256;
#define RING_R 2
constexpr int K2_R = RING_R;
constexpr int K2_SLOT = 1536;
constexpr int O_RING = 0;
constexpr int O_ITEMS = K2_NW * K2_R * K2_SLOT;
constexpr int O_PERM = O_ITEMS + (K2_CAP + 1) * 32;
constexpr int O_SKEY = O_PERM + K2_ICAP * 2;
constexpr int O_NOFF = O_SKEY + K2_ICAP * 4;
constexpr int O_HEAD = O_NOFF + 128 * 4;
constexpr int O_WSUM = O_HEAD + K2_NG * 4;
constexpr int K2_SMEM = O_WSUM + 64;
static_assert(K2_SMEM <= 163840, "LDS");
static_assert(K2_ICAP % 8 == 0 && K2_BN % 4 == 0 && K2_BN <= 128, "align");
constexpr int K2_RND = (K2_CAP + K2_THREADS - 1) / K2_THREADS;
constexpr unsigned SRC_MASK = 0x3fffffu;
constexpr unsigned F_FIN = 0x80000000u, F_SPLIT = 0x40000000u;
#define OUT_AUX 2

DI void masked_zero8(float* d, unsigned long long mask) {
    unsigned long long sv;
    asm volatile("s_and_saveexec_b64 %8, %9\n\ts_cbranch_execz .Lmy%=\n\t"
                 "v_mov_b32 %0, 0\n\tv_mov_b32 %1, 0\n\tv_mov_b32 %2, 0\n\tv_mov_b32 %3, 0\n\t"
                 "v_mov_b32 %4, 0\n\tv_mov_b32 %5, 0\n\tv_mov_b32 %6, 0\n\tv_mov_b32 %7, 0\n"
                 ".Lmy%=:\n\ts_mov_b64 exec, %8"
                 : "+v"(d[0]), "+v"(d[1]), "+v"(d[2]), "+v"(d[3]), "+v"(d[4]), "+v"(d[5]), "+v"(d[6]), "+v"(d[7]), "=&s"(sv)
                 : "s"(mask) : "scc");
}

DI void k2_generic(const unsigned char* __restrict__ svb, const unsigned char* __restrict__ TP,
                   const unsigned short* __restrict__ wrbf, const Rec* __restrict__ rec, const unsigned* __restrict__ tab,
                   unsigned char* __restrict__ outb, int bin, int hf, int node0, int nn, int nchunks) {
    const int lane = threadIdx.x & 63;
    const int w = __builtin_amdgcn_readfirstlane(threadIdx.x >> 6);
    const int gC = lane >> 5, l32 = lane & 31;
    const int ncp = (nchunks + 3) & ~3;
    const size_t co = (size_t)(hf * 128 + l32 * 4);
    float wr[3][4][2];
#pragma unroll
    for (int sg = 0; sg < 3; ++sg)
#pragma unroll
        for (int k = 0; k < 4; ++k) {
            const unsigned u = *reinterpret_cast<const unsigned*>(wrbf + k * D3 + sg * D + 64 * hf + 2 * l32);
            wr[sg][k][0] = bf_lo(u); wr[sg][k][1] = bf_hi(u);
        }
#pragma unroll 1
    for (int j = w; j < nn; j += K2_NW) {
        float acc[8];
#pragma unroll
        for (int r = 0; r < 8; ++r) acc[r] = 0.f;
#pragma unroll 1
        for (int c = 0; c < nchunks; ++c) {
            const unsigned ent = tab[(size_t)bin * ncp + c];
            const int cnt = (int)(ent >> 16);
            const Rec* rp = rec + (size_t)c * CH + (ent & 0xffffu);
#pragma unroll 1
            for (int b = 0; b < cnt; b += 64) {
                const bool in = b + lane < cnt;
                const unsigned key = in ? rp[b + lane].key : 0u;
                unsigned long long m = __ballot(in && (key >> 22) == (unsigned)j);
#pragma unroll 1
                while (m) {
                    const int i0 = __builtin_ctzll(m);
                    m &= m - 1;
                    const int i1 = m ? __builtin_ctzll(m) : -1;
                    m &= m - 1;
                    const int im = gC ? i1 : i0;
                    if (im >= 0) {
                        const u32x4 rr = reinterpret_cast<const u32x4*>(rp + b + im)[0], kk = reinterpret_cast<const u32x4*>(rp + b + im)[1];
                        const size_t n = rr[0] & SRC_MASK;
                        const unsigned* pp = reinterpret_cast<const unsigned*>(TP + n * 1024 + co);
                        const unsigned* vp = reinterpret_cast<const unsigned*>(svb + n * 1024 + co);
                        const unsigned ps = pp[0], pv = pp[64], pr = pp[128], vx = vp[64], vy = vp[128], vz = vp[192];
#pragma unroll
                        for (int q = 0; q < 2; ++q) {
                            float wf[3];
#pragma unroll
                            for (int sg = 0; sg < 3; ++sg)
                                wf[sg] = __uint_as_float(kk[0]) * wr[sg][0][q] + __uint_as_float(kk[1]) * wr[sg][1][q] +
                                         __uint_as_float(kk[2]) * wr[sg][2][q] + __uint_as_float(kk[3]) * wr[sg][3][q];
                            const float mv = (q ? bf_hi(pv) : bf_lo(pv)) * wf[1], mr = (q ? bf_hi(pr) : bf_lo(pr)) * wf[2];
                            acc[q] += (q ? bf_hi(ps) : bf_lo(ps)) * wf[0];
                            acc[2 + q] += mv * (q ? bf_hi(vx) : bf_lo(vx)) + mr * __uint_as_float(rr[1]);
                            acc[4 + q] += mv * (q ? bf_hi(vy) : bf_lo(vy)) + mr * __uint_as_float(rr[2]);
                            acc[6 + q] += mv * (q ? bf_hi(vz) : bf_lo(vz)) + mr * __uint_as_float(rr[3]);
                        }
                    }
                }
            }
        }
#pragma unroll
        for (int r = 0; r < 8; ++r) acc[r] += __shfl_xor(acc[r], 32, 64);
        if (gC == 0) {
            const size_t n = (size_t)(node0 + j);
            const unsigned* bp = reinterpret_cast<const unsigned*>(svb + n * 1024 + co);
            unsigned* op = reinterpret_cast<unsigned*>(outb + n * 1024 + co);
#pragma unroll
            for (int r = 0; r < 4; ++r) {
                const unsigned b = bp[64 * r];
                op[64 * r] = pack_bf16(bf_lo(b) + acc[2 * r], bf_hi(b) + acc[2 * r + 1]);
            }
        }
    }
}

#define K2_MINW 1
__global__ __launch_bounds__(K2_THREADS, K2_MINW) void painn_k2(const unsigned char* __restrict__ svb,
                                                       const unsigned char* __restrict__ TP,
                                                       const unsigned short* __restrict__ wrbf,
                                                       const Rec* __restrict__ rec,
                                                       const unsigned* __restrict__ tab,
                                                       unsigned char* __restrict__ outb, int N, int BN, int NB, int nchunks) {
    extern __shared__ __attribute__((aligned(16))) char smem[];
    const int tid = threadIdx.x;
    const int lane = tid & 63;
    const int w = __builtin_amdgcn_readfirstlane(tid >> 6);
    const int hf = blockIdx.x & 1;
    const int bin = blockIdx.x >> 1;
    STAMP2(0); STAMPX(2048);
    const int node0 = bin * BN;
    const int nn = min(BN, N - node0);
    unsigned* hist = reinterpret_cast<unsigned*>(smem + O_RING);
    u32x4* items = reinterpret_cast<u32x4*>(smem + O_ITEMS);
    unsigned short* perm = reinterpret_cast<unsigned short*>(smem + O_PERM);
    unsigned* skey = reinterpret_cast<unsigned*>(smem + O_SKEY);
    unsigned* noff = reinterpret_cast<unsigned*>(smem + O_NOFF);
    unsigned* headinfo = reinterpret_cast<unsigned*>(smem + O_HEAD);
    unsigned* wsum = reinterpret_cast<unsigned*>(smem + O_WSUM);

    const __amdgpu_buffer_rsrc_t rTP = __builtin_amdgcn_make_buffer_rsrc((void*)TP, 0, (unsigned)N * 1024u, 0x00020000);
    const __amdgpu_buffer_rsrc_t rSV = __builtin_amdgcn_make_buffer_rsrc((void*)svb, 0, (unsigned)N * 1024u, 0x00020000);
    const __amdgpu_buffer_rsrc_t rOut = __builtin_amdgcn_make_buffer_rsrc((void*)outb, 0, (unsigned)N * 1024u, 0x00020000);
    const __amdgpu_buffer_rsrc_t rRec =
        __builtin_amdgcn_make_buffer_rsrc((void*)rec, 0, (unsigned)nchunks * CH * 32u, 0x00020000);

    const int ncp = (nchunks + 3) & ~3;
    u32x4 e = {0u, 0u, 0u, 0u};
    if (4 * lane < nchunks) e = *reinterpret_cast<const u32x4*>(tab + (size_t)bin * ncp + 4 * lane);
#pragma unroll
    for (int q = 1; q < 4; ++q) e[q] = 4 * lane + q < nchunks ? e[q] : 0u;
    unsigned wpk[3][4];
#pragma unroll
    for (int sg = 0; sg < 3; ++sg)
#pragma unroll
        for (int k = 0; k < 4; ++k)
            wpk[sg][k] = *reinterpret_cast<const unsigned*>(wrbf + k * D3 + sg * D + 64 * hf + 2 * (lane & 31));
    if (tid < K2_NW * K2_BN / 4) reinterpret_cast<u32x4*>(hist)[tid] = u32x4{0u, 0u, 0u, 0u};
    if (tid < 2) items[2 * K2_CAP + tid] = u32x4{0u, 0u, 0u, 0u};
    if (tid < K2_NG) headinfo[tid] = 0xffffffffu;
    STAMP2(1);
    int T;
    {
        const unsigned c0 = e[0] >> 16, c1 = e[1] >> 16, c2 = e[2] >> 16, c3 = e[3] >> 16;
        const unsigned incl = wave_incl_scan(c0 + c1 + c2 + c3, lane);
        T = __builtin_amdgcn_readlane((int)incl, 63);
        const unsigned o3 = incl - c3, o2 = o3 - c2, o1 = o2 - c1, o0 = o1 - c0;
        const unsigned me = (w & 3) == 0 ? e[0] : (w & 3) == 1 ? e[1] : (w & 3) == 2 ? e[2] : e[3];
        const unsigned mo = (w & 3) == 0 ? o0 : (w & 3) == 1 ? o1 : (w & 3) == 2 ? o2 : o3;
        const unsigned ib = lds_addr(items);
        if (T <= K2_CAP && nchunks <= K2_MAXCH && BN <= K2_BN) {
#pragma unroll 1
            for (int l = w >> 2; 4 * l + (w & 3) < nchunks; l += K2_NW / 4) {
                const unsigned ee = (unsigned)__builtin_amdgcn_readlane((int)me, l);
                const unsigned o = (unsigned)__builtin_amdgcn_readlane((int)mo, l);
                const unsigned cnt2 = 2u * (ee >> 16), gb = ((unsigned)(4 * l + (w & 3)) * CH + (ee & 0xffffu)) * 32u;
#pragma unroll 1
                for (unsigned i = 0; i < cnt2; i += 64u)
                    if (i + (unsigned)lane < cnt2) dma16(rRec, gb + (i + (unsigned)lane) * 16u, ib + o * 32u + i * 16u);
            }
        }
    }
    if (T > K2_CAP || nchunks > K2_MAXCH || BN > K2_BN) {
        asm volatile("s_waitcnt vmcnt(0)" ::: "memory");
        k2_generic(svb, TP, wrbf, rec, tab, outb, bin, hf, node0, nn, nchunks);
        return;
    }
    const int Tc = T;
    const int M = Tc + nn;
    const int L = (M + K2_NG - 1) / K2_NG;
    asm volatile("s_waitcnt vmcnt(0)" ::: "memory");
    __syncthreads();
    STAMP2(2);
    unsigned* myhist = hist + w * K2_BN;
    unsigned rdk[K2_RND], rkey[K2_RND];
#pragma unroll
    for (int r = 0; r < K2_RND; ++r)
        if (K2_THREADS * r + tid < Tc) {
            rkey[r] = items[2 * (K2_THREADS * r + tid)][0];
            const unsigned dl = rkey[r] >> 22;
            rdk[r] = dl | (atomicAdd(&myhist[dl], 1u) << 16);
        }
    __syncthreads();
    STAMP2(4);
    if (w < 2) {
        unsigned ntot = 0;
        if (tid < K2_BN) {
            unsigned h[K2_NW];
#pragma unroll
            for (int ww = 0; ww < K2_NW; ++ww) h[ww] = hist[ww * K2_BN + tid];
#pragma unroll
            for (int ww = 0; ww < K2_NW; ++ww) { hist[ww * K2_BN + tid] = ntot; ntot += h[ww]; }
        }
        const unsigned len = tid < nn ? ntot + 1u : 0u;
        const unsigned nincl = wave_incl_scan(len, lane);
        if (lane == 63) wsum[w] = nincl;
        noff[tid] = (nincl - len) | (ntot << 16);
    }
    __syncthreads();
    STAMP2(5);
    const unsigned w0tot = wsum[0];
    const unsigned Mdiv = 0xffffffffu / (unsigned)L + 1u;
#pragma unroll
    for (int r = 0; r < K2_RND; ++r)
        if (K2_THREADS * r + tid < Tc) {
            const unsigned dl = rdk[r] & 0xffffu;
            const unsigned p = (noff[dl] & 0xffffu) + (dl >= 64u ? w0tot : 0u) + myhist[dl] + (rdk[r] >> 16);
            perm[p] = (unsigned short)(K2_THREADS * r + tid);
            skey[p] = rkey[r] & SRC_MASK;
        }
    if (tid < nn) {
        const unsigned no = noff[tid];
        const unsigned p0 = (no & 0xffffu) + (tid >= 64 ? w0tot : 0u), pf = p0 + (no >> 16);
        const unsigned g0 = L == 1 ? p0 : __umulhi(p0, Mdiv), gf = L == 1 ? pf : __umulhi(pf, Mdiv);
        perm[pf] = (unsigned short)K2_CAP;
        skey[pf] = (unsigned)(node0 + tid) | F_FIN | (g0 < gf ? F_SPLIT : 0u);
        if (g0 < gf) headinfo[gf] = g0;
    }
    if (tid >= 128 && M + (tid - 128) < K2_NG * L + 16) {
        perm[M + tid - 128] = (unsigned short)K2_CAP;
        skey[M + tid - 128] = 0u;
    }
    float wr[3][4][2];
#pragma unroll
    for (int sg = 0; sg < 3; ++sg)
#pragma unroll
        for (int k = 0; k < 4; ++k) { wr[sg][k][0] = bf_lo(wpk[sg][k]); wr[sg][k][1] = bf_hi(wpk[sg][k]); }
    __syncthreads();
    STAMP2(9);

    float acc[8], accH[8];
#pragma unroll
    for (int r = 0; r < 8; ++r) { acc[r] = 0.f; accH[r] = 0.f; }
    unsigned hnode = 0xffffffffu;
    const int gC = lane >> 5, l32 = lane & 31;
    const int gD = lane >= 24 ? 1 : 0, lD = lane - 24 * gD;
    const unsigned short* pp = perm + (2 * w + gC) * L;
    const unsigned* pkC = skey + (2 * w + gC) * L;
    const unsigned* pkD = skey + (2 * w + gD) * L;
    const unsigned offP = (unsigned)((lD >> 3) * 256 + hf * 128 + (lD & 7) * 16);
    const unsigned offS = lD < 8 ? (unsigned)(768 + hf * 128 + lD * 16) : 0x80000000u;
    const unsigned offV = offP + 256u;
    const unsigned ringb = lds_addr(smem + O_RING + w * (K2_R * K2_SLOT));
    const char* ringp = smem + O_RING + w * (K2_R * K2_SLOT) + gC * 384 + l32 * 4;

    auto issue = [&](unsigned key, int slot) LAM {
        const unsigned ro = (key & SRC_MASK) * 1024u;
        if (lane < 48) {
            dma16g(rTP, ro + ((key & F_FIN) ? offS : offP), ringb + (unsigned)slot * K2_SLOT);
            dma16g(rSV, ro + offV, ringb + (unsigned)slot * K2_SLOT + 768u);
        }
    };
    auto compute = [&](int slot, unsigned q, unsigned key) LAM {
        const u32x4 rr = items[2 * q], kk = items[2 * q + 1];
        const unsigned* dp = reinterpret_cast<const unsigned*>(ringp + slot * K2_SLOT);
        asm volatile("s_waitcnt vmcnt(%0)" :: "n"(2 * (K2_R - 1)) : "memory");
        const unsigned ps = dp[0], pv = dp[32], pr = dp[64], vx = dp[192], vy = dp[224], vz = dp[256];
        const float hx = __uint_as_float(rr[1]), hy = __uint_as_float(rr[2]), hz = __uint_as_float(rr[3]);
        const float c0 = __uint_as_float(kk[0]), c1 = __uint_as_float(kk[1]), c2 = __uint_as_float(kk[2]), c3 = __uint_as_float(kk[3]);
        float u[8];
#pragma unroll
        for (int j = 0; j < 2; ++j) {
            u[j] = j ? bf_hi(ps) : bf_lo(ps);
            u[2 + j] = j ? bf_hi(vx) : bf_lo(vx);
            u[4 + j] = j ? bf_hi(vy) : bf_lo(vy);
            u[6 + j] = j ? bf_hi(vz) : bf_lo(vz);
            const float fv = j ? bf_hi(pv) : bf_lo(pv), fr = j ? bf_hi(pr) : bf_lo(pr);
            float wfs, wfv, wfr;
            asm("v_mul_f32 %0, %3, %7\n\tv_mul_f32 %1, %3, %11\n\tv_mul_f32 %2, %3, %15\n\t"
                "v_fmac_f32 %0, %4, %8\n\tv_fmac_f32 %1, %4, %12\n\tv_fmac_f32 %2, %4, %16\n\t"
                "v_fmac_f32 %0, %5, %9\n\tv_fmac_f32 %1, %5, %13\n\tv_fmac_f32 %2, %5, %17\n\t"
                "v_fmac_f32 %0, %6, %10\n\tv_fmac_f32 %1, %6, %14\n\tv_fmac_f32 %2, %6, %18\n\t"
                "v_mul_f32 %1, %19, %1\n\tv_mul_f32 %2, %20, %2"
                : "=&v"(wfs), "=&v"(wfv), "=&v"(wfr)
                : "v"(c0), "v"(c1), "v"(c2), "v"(c3), "v"(wr[0][0][j]), "v"(wr[0][1][j]), "v"(wr[0][2][j]), "v"(wr[0][3][j]),
                  "v"(wr[1][0][j]), "v"(wr[1][1][j]), "v"(wr[1][2][j]), "v"(wr[1][3][j]),
                  "v"(wr[2][0][j]), "v"(wr[2][1][j]), "v"(wr[2][2][j]), "v"(wr[2][3][j]), "v"(fv), "v"(fr));
            asm("v_fmac_f32 %0, %4, %5\n\tv_fmac_f32 %1, %6, %8\n\tv_fmac_f32 %2, %6, %9\n\tv_fmac_f32 %3, %6, %10\n\t"
                "v_fmac_f32 %1, %7, %11\n\tv_fmac_f32 %2, %7, %12\n\tv_fmac_f32 %3, %7, %13"
                : "+v"(acc[j]), "+v"(acc[2 + j]), "+v"(acc[4 + j]), "+v"(acc[6 + j])
                : "v"(u[j]), "v"(wfs), "v"(wfv), "v"(wfr), "v"(u[2 + j]), "v"(u[4 + j]), "v"(u[6 + j]), "v"(hx), "v"(hy), "v"(hz));
        }
        const bool fin = (int)key < 0;
        if (__ballot(fin) != 0ull) {
            float t[8];
#pragma unroll
            for (int r = 0; r < 8; ++r) t[r] = acc[r] + u[r];
            const bool split = (key & F_SPLIT) != 0u;
            const unsigned oo = (fin && !split) ? (key & SRC_MASK) * 1024u + (unsigned)(hf * 128 + l32 * 4) : 0x80000000u;
#pragma unroll
            for (int r = 0; r < 4; ++r)
                __builtin_amdgcn_raw_buffer_store_b32(pack_bf16(t[2 * r], t[2 * r + 1]), rOut, oo + (unsigned)r * 256u, 0, OUT_AUX);
            masked_copy8(accH, t, __ballot(fin && split));
            hnode = (fin && split) ? (key & SRC_MASK) : hnode;
            masked_zero8(acc, __ballot(fin));
        }
    };

    {
#pragma unroll
        for (int k = 0; k < K2_R - 1; ++k) issue(pkD[k], k);
        unsigned kD = pkD[K2_R - 1], q = pp[0], kC = pkC[0];
        int slot = 0, fslot = K2_R - 1;
#pragma unroll 1
        for (int it = 0; it < L; ++it) {
            const unsigned kDn = pkD[it + K2_R], qn = pp[it + 1], kCn = pkC[it + 1];
            issue(kD, fslot);
            compute(slot, q, kC);
            fslot = slot;
            slot = slot + 1 == K2_R ? 0 : slot + 1;
            kD = kDn; q = qn; kC = kCn;
        }
        asm volatile("s_waitcnt vmcnt(0)" ::: "memory");
    }
    STAMP2(10);
    __syncthreads();
    STAMP2(11);
    {
        float* tl = reinterpret_cast<float*>(smem + O_RING);
        const int G = 2 * w + gC;
#pragma unroll
        for (int r = 0; r < 8; ++r) tl[(G * 8 + r) * 32 + l32] = acc[r];
        __syncthreads();
        const unsigned g0 = headinfo[G];
        if (g0 != 0xffffffffu) {
            float t[8];
#pragma unroll
            for (int r = 0; r < 8; ++r) t[r] = tl[(g0 * 8 + r) * 32 + l32];
#pragma unroll 1
            for (int gg = (int)g0 + 1; gg < G; ++gg) {
#pragma unroll
                for (int r = 0; r < 8; ++r) t[r] += tl[(gg * 8 + r) * 32 + l32];
            }
            const unsigned oo = hnode * 1024u + (unsigned)(hf * 128 + l32 * 4);
#pragma unroll
            for (int r = 0; r < 4; ++r)
                __builtin_amdgcn_raw_buffer_store_b32(pack_bf16(t[2 * r] + accH[2 * r], t[2 * r + 1] + accH[2 * r + 1]), rOut,
                                                      oo + (unsigned)r * 256u, 0, OUT_AUX);
        }
    }
    STAMP2(13);
}

extern "C" void kernel_launch(void* const* d_in, const int* in_sizes, int n_in, void* d_out, int out_size,
                              void* d_ws, size_t ws_size, hipStream_t stream) {
    const unsigned short* sv = (const unsigned short*)d_in[0];
    const unsigned char* pos = (const unsigned char*)d_in[1];
    const int* src = (const int*)d_in[2];
    const int* dst = (const int*)d_in[3];
    const unsigned short* freqs = (const unsigned short*)d_in[4];
    const unsigned short* w1 = (const unsigned short*)d_in[5];
    const unsigned short* w2 = (const unsigned short*)d_in[6];
    const unsigned short* wrbf = (const unsigned short*)d_in[7];
    const int N = in_sizes[0] / (4 * D);
    const int E = in_sizes[2];

    const int BN = N > 2 * MAXNB ? (N + MAXNB - 1) / MAXNB : 2;
    const int NB = (N + BN - 1) / BN;
    const int nchunks = (E + CH - 1) / CH;
    const int ngemm = (N + G_ROWS - 1) / G_ROWS;
    const unsigned binmagic = (unsigned)(0xffffffffull / (unsigned)BN + 1ull);

    char* ws = (char*)d_ws;
    unsigned char* TP = (unsigned char*)ws;
    size_t off = ((size_t)N * 1024 + 255) & ~(size_t)255;
    Rec* rec = (Rec*)(ws + off);
    off += (size_t)nchunks * CH * sizeof(Rec);
    unsigned* tab = (unsigned*)(ws + off);

    (void)hipFuncSetAttribute((const void*)painn_k1, hipFuncAttributeMaxDynamicSharedMemorySize, K1_SMEM);
    painn_k1<<<ngemm + nchunks, K1_THREADS, K1_SMEM, stream>>>(sv, pos, src, dst, freqs, w1, w2, TP, rec, tab, N, E,
                                                                BN, NB, nchunks, ngemm, binmagic);
    (void)hipFuncSetAttribute((const void*)painn_k2, hipFuncAttributeMaxDynamicSharedMemorySize, K2_SMEM);
    painn_k2<<<NB * 2, K2_THREADS, K2_SMEM, stream>>>((const unsigned char*)sv, TP, wrbf, rec, tab, (unsigned char*)d_out, N, BN, NB,
                                                      nchunks);
}
```
